```python
import torch

def _softmax(x, axis=-1):
    return torch.softmax(x, dim=axis)
def _gelu(x):
    return torch.nn.functional.gelu(x, approximate="tanh")
def _silu(x):
    return torch.nn.functional.silu(x)
def _sigmoid(x):
    return torch.sigmoid(x)
def _layernorm(x, axis=-1, eps=1e-5):
    m = x.mean(dim=axis, keepdim=True)
    v = x.var(dim=axis, keepdim=True, unbiased=False)
    return (x - m) / torch.sqrt(v + eps)
def _scatter_add(tgt, idx, src, axis=0):
    return tgt.clone().index_add_(axis, idx, src)
def _segment_sum(src, seg, n):
    out = torch.zeros((n,) + src.shape[1:], dtype=src.dtype)
    return out.index_add_(0, seg, src)
def _pad(x, pw):
    flat = tuple(v for pair in reversed(pw) for v in pair)
    return torch.nn.functional.pad(x, flat)

kernel_name = 'gated_mix__ssm_conv_h3__grouped_kv_mqa__prompt'

def setup_inputs():

    _w = (lambda a: (lambda t: t.to(torch.bfloat16) if t.is_floating_point() else t)(torch.as_tensor(a)))
    def _A_setup():
        B, S, D = 1, 2048, 512
        ks = 4
        return {
            "q": _w((lambda *s: torch.randn(*s, dtype=torch.bfloat16) * 0.05)(B, S, D)),
            "k": _w((lambda *s: torch.randn(*s, dtype=torch.bfloat16) * 0.2)(B, S, D)),
            "v": _w((lambda *s: torch.randn(*s, dtype=torch.bfloat16) * 0.2)(B, S, D)),
            "shift_k": _w((lambda *s: torch.randn(*s, dtype=torch.bfloat16) * 0.2)(D, ks) * 0.5),
            "ssm_k": _w((lambda *s: torch.randn(*s, dtype=torch.bfloat16) * 0.8)(D, S) * (1.0 / S ** 0.5)),
            "ssm_D": _w((lambda *s: torch.randn(*s, dtype=torch.bfloat16) * 0.2)(D)),
        }
    _A_in = _A_setup()

    def _B_setup():
        B, S, D, H = 1, 2048, 512, 32
        Dh = D // H
        return {
            "x": _w((lambda *s: torch.randn(*s, dtype=torch.bfloat16) * 0.05)(B, S, D)),
            "wq": _w((lambda *s: torch.randn(*s, dtype=torch.bfloat16) * 0.8)(D, H * Dh)),
            "wk": _w((lambda *s: torch.randn(*s, dtype=torch.bfloat16) * 0.8)(D, Dh)),
            "wv": _w((lambda *s: torch.randn(*s, dtype=torch.bfloat16) * 0.05)(D, Dh)),
            "wo": _w((lambda *s: torch.randn(*s, dtype=torch.bfloat16) * 0.05)(H * Dh, D)),
        }
    _B_in = _B_setup()

    return {
        "A_q": _A_in["q"],
        "A_k": _A_in["k"],
        "A_v": _A_in["v"],
        "A_shift_k": _A_in["shift_k"],
        "A_ssm_k": _A_in["ssm_k"],
        "A_ssm_D": _A_in["ssm_D"],
        "B_x": _B_in["x"],
        "B_wq": _B_in["wq"],
        "B_wk": _B_in["wk"],
        "B_wv": _B_in["wv"],
        "B_wo": _B_in["wo"],
        "gate_w": _w((lambda *s: torch.randn(*s, dtype=torch.bfloat16) * 0.05)(512) * 16.0),
    }

def reference(A_q, A_k, A_v, A_shift_k, A_ssm_k, A_ssm_D, B_x, B_wq, B_wk, B_wv, B_wo, gate_w):
    def _A_ref(q, k, v, shift_k, ssm_k, ssm_D):
        B, S, D = q.shape
        ks = shift_k.shape[-1]
        k_pad = _pad(k, ((0, 0), (ks - 1, 0), (0, 0)))
        k_shift = torch.zeros_like(k)
        for t in range(ks):
            k_shift = k_shift + k_pad[:, t:t + S, :] * shift_k[:, ks - 1 - t][None, None, :]
        kv = k_shift * v
        L = 2 * S
        kv_pad = _pad(kv, ((0, 0), (0, S), (0, 0)))
        kf_pad = _pad(ssm_k, ((0, 0), (0, S)))
        kv_f = (lambda x, axes=(-2,-1): torch.fft.rfft2(x.float(), dim=axes))(kv_pad[:, None, :, :], axes=(1, 2))
        kf_f = (lambda x, axes=(-2,-1): torch.fft.rfft2(x.float(), dim=axes))(kf_pad[:, None, :], axes=(1, 2))
        kf_f = (lambda x, *d: x.permute(*d))(kf_f, 1, 2, 0)
        y_f = kv_f * kf_f[None, :, :, :]
        y = (lambda x, s, axes=(-2,-1): torch.fft.irfft2(x, s=s, dim=axes).to(torch.bfloat16))(y_f, (1, L), axes=(1, 2))[:, 0, :S, :]
        y = y + kv * ssm_D[None, None, :]
        return q * y

    def _B_ref(x, wq, wk, wv, wo):
        B, S, D = x.shape
        H = 32
        Dh = D // H
        q = (lambda x, *d: x.permute(*d))((x @ wq).reshape(B, S, H, Dh), 0, 2, 1, 3)
        k = x @ wk
        v = x @ wv
        scores = torch.einsum("bhqd,bkd->bhqk", q, k) * (Dh ** -0.5)
        p = _softmax(scores, axis=-1)
        o = torch.einsum("bhqk,bkd->bhqd", p, v)
        return (lambda x, *d: x.permute(*d))(o, 0, 2, 1, 3).reshape(B, S, H * Dh) @ wo

    x = A_q
    g = _sigmoid((x * gate_w).sum(-1, keepdims=True))
    a = _A_ref(x, A_k, A_v, A_shift_k, A_ssm_k, A_ssm_D)
    b = _B_ref(x, B_wq, B_wk, B_wv, B_wo)
    return g * a + (1 - g) * b
```

```cpp
#include <hip/hip_runtime.h>
#include <hip/hip_bf16.h>
#include <stdint.h>

typedef unsigned short u16;
typedef unsigned int u32;
typedef __attribute__((ext_vector_type(8))) __bf16 bf16x8;
typedef __attribute__((ext_vector_type(4))) short s16x4;
typedef __attribute__((ext_vector_type(16))) float f32x16;
typedef __attribute__((ext_vector_type(4))) float f32x4;
typedef __attribute__((ext_vector_type(4))) u32 u32x4;
typedef __attribute__((ext_vector_type(2))) u32 u32x2;
typedef __attribute__((ext_vector_type(2))) float f32x2;
typedef __attribute__((ext_vector_type(2))) __bf16 bf16x2;
typedef __attribute__((address_space(3))) s16x4* lds_s16x4_p;
typedef __attribute__((address_space(3))) void* lds_void_p;
typedef const __attribute__((address_space(1))) void* glb_cvoid_p;

#define DEV __device__ __forceinline__

constexpr int SEQ = 2048;
constexpr int DM = 512;

constexpr size_t WS_QF = 0;
constexpr size_t WS_OF = WS_QF + 2097152;
constexpr size_t WS_UR = WS_OF + 2097152;
constexpr size_t WS_YT = WS_UR + 2097152;
constexpr size_t WS_KF = WS_YT + 2097152;
constexpr size_t WS_VF = WS_KF + 65536;
constexpr size_t WS_G = WS_VF + 65536;
constexpr size_t WS_KM = WS_G + 8192;

DEV float bf_lo(u32 v) { return __builtin_bit_cast(float, v << 16); }
DEV float bf_hi(u32 v) { return __builtin_bit_cast(float, v & 0xffff0000u); }
DEV u32 pk_bf16(float a, float b) {
    f32x2 f = {a, b};
    bf16x2 r = __builtin_convertvector(f, bf16x2);
    return __builtin_bit_cast(u32, r);
}
DEV void dma16(const void* g, char* lds) {
    __builtin_amdgcn_global_load_lds((glb_cvoid_p)g, (lds_void_p)lds, 16, 0, 0);
}
DEV bf16x8 as_bf8(u32x4 v) { return __builtin_bit_cast(bf16x8, v); }
DEV u32x4 lds_rd128(const char* p) { return *(const u32x4*)p; }
DEV u32x2 lds_tr(const char* p) {
    s16x4 v = __builtin_amdgcn_ds_read_tr16_b64_v4i16((lds_s16x4_p)(p));
    return __builtin_bit_cast(u32x2, v);
}
DEV bf16x8 cat_tr(u32x2 a, u32x2 b) {
    u32x4 r = {a[0], a[1], b[0], b[1]};
    return as_bf8(r);
}
DEV float xor_lane_f(float v, int m) { return __shfl_xor(v, m, 64); }

constexpr int K1_X = 0;
constexpr int K1_W = 65536;
constexpr int K1_WKV = 131072;
constexpr int K1_EX = 147456;
constexpr int K1_LDS = 163840;

__global__ __launch_bounds__(512) void k1_prep(const u16* __restrict__ x, const u16* __restrict__ kin,
                                               const u16* __restrict__ vin, const u16* __restrict__ shift_k,
                                               const u16* __restrict__ wq, const u16* __restrict__ wk,
                                               const u16* __restrict__ wv, const u16* __restrict__ gate_w,
                                               char* __restrict__ ws) {
    extern __shared__ __attribute__((aligned(16))) char smem[];
    const int b = blockIdx.x;
    const int sb = (b & 7) * 4 + (b >> 6);
    const int nb = (b >> 3) & 7;
    const int tid = threadIdx.x;
    const int lane = tid & 63;
    const int w = __builtin_amdgcn_readfirstlane(tid >> 6);
    const int hh = lane >> 5;

    u32x4 gw = {0, 0, 0, 0};
    if (w >= 4) {
        const int wl = w - 4;
        const u16* xg = x + (size_t)(64 * sb) * DM;
#pragma unroll
        for (int i = 0; i < 16; ++i) {
            const int row = wl * 16 + i;
            dma16(xg + row * DM + 8 * (lane ^ (row & 15)), smem + K1_X + row * 1024);
        }
        const u16* wg = wq + nb * 64;
#pragma unroll
        for (int i = 0; i < 16; ++i) {
            const int k0 = (wl * 16 + i) * 8;
            const int k = k0 + (lane >> 3);
            const int cp = lane & 7;
            dma16(wg + k * DM + 8 * (cp ^ (4 * ((k >> 1) & 1))), smem + K1_W + k0 * 128);
        }
        const u16* wkv = (nb & 1) ? wv : wk;
#pragma unroll
        for (int i = 0; i < 4; ++i) {
            const int c = wl * 4 + i;
            dma16(wkv + c * 512 + lane * 8, smem + K1_WKV + c * 1024);
        }
    } else {
        const int tb = lane & 7, dp = lane >> 3;
        const int d0 = 64 * nb + 16 * w + 2 * dp;
        const int t0 = 64 * sb + 8 * tb;
        u32 kr[11], vr[8];
#pragma unroll
        for (int i = 0; i < 11; ++i) {
            const int t = t0 - 3 + i;
            kr[i] = (t >= 0) ? *(const u32*)(kin + (size_t)t * DM + d0) : 0u;
        }
#pragma unroll
        for (int i = 0; i < 8; ++i) vr[i] = *(const u32*)(vin + (size_t)(t0 + i) * DM + d0);
        const u32x4 sw = *(const u32x4*)(shift_k + d0 * 4);
        const float w00 = bf_lo(sw[0]), w01 = bf_hi(sw[0]), w02 = bf_lo(sw[1]), w03 = bf_hi(sw[1]);
        const float w10 = bf_lo(sw[2]), w11 = bf_hi(sw[2]), w12 = bf_lo(sw[3]), w13 = bf_hi(sw[3]);
        float u0[8], u1[8];
#pragma unroll
        for (int i = 0; i < 8; ++i) {
            const float a = bf_lo(kr[i + 3]) * w00 + bf_lo(kr[i + 2]) * w01 + bf_lo(kr[i + 1]) * w02 + bf_lo(kr[i]) * w03;
            const float c = bf_hi(kr[i + 3]) * w10 + bf_hi(kr[i + 2]) * w11 + bf_hi(kr[i + 1]) * w12 + bf_hi(kr[i]) * w13;
            u0[i] = a * bf_lo(vr[i]);
            u1[i] = c * bf_hi(vr[i]);
        }
        u16* uR = (u16*)(ws + WS_UR);
        const int i0 = 2040 - t0;
        u32x4 o0 = {pk_bf16(u0[7], u0[6]), pk_bf16(u0[5], u0[4]), pk_bf16(u0[3], u0[2]), pk_bf16(u0[1], u0[0])};
        u32x4 o1 = {pk_bf16(u1[7], u1[6]), pk_bf16(u1[5], u1[4]), pk_bf16(u1[3], u1[2]), pk_bf16(u1[1], u1[0])};
        *(u32x4*)(uR + (size_t)d0 * SEQ + i0) = o0;
        *(u32x4*)(uR + (size_t)(d0 + 1) * SEQ + i0) = o1;
    }
    gw = *(const u32x4*)(gate_w + lane * 8);
    asm volatile("s_waitcnt vmcnt(0)" ::: "memory");
    __syncthreads();

    {
        const int row = 8 * nb + w;
        const u32x4 xv = lds_rd128(smem + K1_X + row * 1024 + 16 * (lane ^ (row & 15)));
        float s = 0.f;
#pragma unroll
        for (int i = 0; i < 4; ++i) s += bf_lo(xv[i]) * bf_lo(gw[i]) + bf_hi(xv[i]) * bf_hi(gw[i]);
#pragma unroll
        for (int m = 1; m < 64; m <<= 1) s += xor_lane_f(s, m);
        if (lane == 0) ((float*)(ws + WS_G))[64 * sb + row] = 1.f / (1.f + __expf(-s));
    }

    if (w == 7) {
        const int kg = nb >> 1;
        const int g = lane >> 4, c16 = lane & 15;
        const int row = 16 * kg + c16;
        const int q = c16 >> 2, p = lane & 3;
        f32x4 acc = {0.f, 0.f, 0.f, 0.f};
        const char* xb = smem + K1_X + row * 1024;
        const char* wb = smem + K1_WKV + (8 * g + q) * 32 + 8 * p;
        if (nb & 1) {
#pragma unroll
            for (int i = 0; i < 16; ++i) {
                const bf16x8 xf = as_bf8(lds_rd128(xb + 16 * ((4 * i + g) ^ c16)));
                const bf16x8 wf = cat_tr(lds_tr(wb + i * 1024), lds_tr(wb + i * 1024 + 128));
                acc = __builtin_amdgcn_mfma_f32_16x16x32_bf16(xf, wf, acc, 0, 0, 0);
            }
            const int T = 2 * sb + (kg >> 1), s = kg & 1;
            u32x2 o = {pk_bf16(acc[0], acc[1]), pk_bf16(acc[2], acc[3])};
            char* dst = ws + WS_VF + (size_t)(T * 2 + s) * 512 + (g & 1) * 256 + c16 * 16 + 8 * (g >> 1);
            *(u32x2*)dst = o;
        } else {
#pragma unroll
            for (int i = 0; i < 16; ++i) {
                const bf16x8 xf = as_bf8(lds_rd128(xb + 16 * ((4 * i + g) ^ c16)));
                const bf16x8 wf = cat_tr(lds_tr(wb + i * 1024), lds_tr(wb + i * 1024 + 128));
                acc = __builtin_amdgcn_mfma_f32_16x16x32_bf16(wf, xf, acc, 0, 0, 0);
            }
            const int T = 2 * sb + (kg >> 1);
            const int key = 16 * (kg & 1) + c16;
            const int rho = (key & 19) | ((key & 4) << 1) | ((key & 8) >> 1);
            u32x2 o = {pk_bf16(acc[0], acc[1]), pk_bf16(acc[2], acc[3])};
            char* dst = ws + WS_KF + (size_t)T * 1024 + (rho + 32 * (g & 1)) * 16 + 8 * (g >> 1);
            *(u32x2*)dst = o;
            float n2 = bf_lo(o[0]) * bf_lo(o[0]) + bf_hi(o[0]) * bf_hi(o[0]) + bf_lo(o[1]) * bf_lo(o[1]) + bf_hi(o[1]) * bf_hi(o[1]);
            n2 += xor_lane_f(n2, 16);
            n2 += xor_lane_f(n2, 32);
#pragma unroll
            for (int m = 1; m < 16; m <<= 1) n2 = fmaxf(n2, xor_lane_f(n2, m));
            if (lane == 0) ((float*)(ws + WS_KM))[4 * sb + kg] = n2;
        }
    }

    {
        const int nt = w & 1, st = (w >> 1) & 1, kh = w >> 2;
        const int r31 = lane & 31;
        const int row = 32 * st + r31;
        const int blk = (lane >> 4) & 1, q = (lane & 15) >> 2, p = lane & 3;
        const char* xb = smem + K1_X + row * 1024 + kh * 512;
        const char* wb = smem + K1_W + (kh * 256 + 8 * hh + q) * 128 + 64 * (nt ^ ((q >> 1) & 1)) + 32 * blk + 8 * p;
        f32x16 acc = {0};
#pragma unroll
        for (int i = 0; i < 16; ++i) {
            const int c = 2 * i + hh;
            const bf16x8 xf = as_bf8(lds_rd128(xb + 16 * ((c & 16) | ((c ^ row) & 15))));
            const bf16x8 wf = cat_tr(lds_tr(wb + i * 2048), lds_tr(wb + i * 2048 + 512));
            acc = __builtin_amdgcn_mfma_f32_32x32x16_bf16(wf, xf, acc, 0, 0, 0);
        }
        char* ex = smem + K1_EX;
        f32x4 s0, s1, k0, k1;
        if (kh == 0) {
            s0 = f32x4{acc[8], acc[9], acc[10], acc[11]};
            s1 = f32x4{acc[12], acc[13], acc[14], acc[15]};
            k0 = f32x4{acc[0], acc[1], acc[2], acc[3]};
            k1 = f32x4{acc[4], acc[5], acc[6], acc[7]};
        } else {
            s0 = f32x4{acc[0], acc[1], acc[2], acc[3]};
            s1 = f32x4{acc[4], acc[5], acc[6], acc[7]};
            k0 = f32x4{acc[8], acc[9], acc[10], acc[11]};
            k1 = f32x4{acc[12], acc[13], acc[14], acc[15]};
        }
        *(f32x4*)(ex + w * 2048 + lane * 16) = s0;
        *(f32x4*)(ex + w * 2048 + 1024 + lane * 16) = s1;
        __syncthreads();
        const f32x4 r0 = *(const f32x4*)(ex + (w ^ 4) * 2048 + lane * 16);
        const f32x4 r1 = *(const f32x4*)(ex + (w ^ 4) * 2048 + 1024 + lane * 16);
        const float sc = 0.25f * 1.4426950408889634f;
        k0 = (k0 + r0) * sc;
        k1 = (k1 + r1) * sc;
        const int head = 4 * nb + 2 * nt + kh;
        u32x4 o = {pk_bf16(k0[0], k0[1]), pk_bf16(k0[2], k0[3]), pk_bf16(k1[0], k1[1]), pk_bf16(k1[2], k1[3])};
        *(u32x4*)(ws + WS_QF + ((size_t)(head * 64 + 2 * sb + st) * 64 + lane) * 16) = o;
    }
}

constexpr int K2_K = 0;
constexpr int K2_V = 65536;
constexpr int K2_ONES = 131072;
constexpr int K2_H = K2_ONES + 1664;
constexpr int K2_U = K2_H + 4 * 4160;
constexpr int K2_LDS = K2_U + 160 * 80;
static_assert(K2_LDS <= 163840, "lds");
static_assert((K2_U & 15) == 0 && (K2_H & 15) == 0, "align");

template <int CH>
DEV void conv_wave(char* smem, int nt, int lane, int ch_global, const u16* __restrict__ ssm_D, char* __restrict__ ws) {
    const int r = lane & 31, hh = lane >> 5;
    const char* hb = smem + K2_H + CH * 8320 + ((r & 1) ? 0 : 4160) + 16 * hh + 4 * (r >> 1);
    const char* ub;
    if (CH == 0) ub = smem + K2_U + (63 - 32 * nt - r) * 80 + 16 * hh;
    else ub = smem + K2_U + 96 * 80 + (32 * nt + r) * 80 + 16 * hh;
    f32x16 acc = {0};
    const int nd = 32 * (nt + 1);
    for (int d0 = 0; d0 < nd; d0 += 8) {
#pragma unroll
        for (int dd = 0; dd < 8; ++dd) {
#pragma unroll
            for (int kb = 0; kb < 2; ++kb) {
                const u32* hp = (const u32*)(hb + 32 * (2 * dd - kb + 1));
                u32x4 a = {hp[0], hp[1], hp[2], hp[3]};
                const char* up = (CH == 0) ? (ub + 80 * dd + 32 * (1 - kb)) : (ub - 80 * dd + 32 * (1 - kb));
                const u32x4 bb = lds_rd128(up);
                acc = __builtin_amdgcn_mfma_f32_32x32x16_bf16(as_bf8(a), as_bf8(bb), acc, 0, 0, 0);
            }
        }
        hb += 32 * 16;
        ub += (CH == 0) ? 80 * 8 : -80 * 8;
    }
    const int beta = 32 * nt + r;
    const char* ublk = (CH == 0) ? (smem + K2_U + (63 - beta) * 80) : (smem + K2_U + 96 * 80 + beta * 80);
    const float Dv = bf_lo((u32)ssm_D[ch_global]);
    u16* yT = (u16*)(ws + WS_YT) + (size_t)ch_global * SEQ + 32 * beta + 4 * hh;
#pragma unroll
    for (int qd = 0; qd < 4; ++qd) {
        const u32x2 uu = *(const u32x2*)(ublk + 2 * (28 - 8 * qd - 4 * hh));
        const float y0 = acc[4 * qd + 0] + Dv * bf_hi(uu[1]);
        const float y1 = acc[4 * qd + 1] + Dv * bf_lo(uu[1]);
        const float y2 = acc[4 * qd + 2] + Dv * bf_hi(uu[0]);
        const float y3 = acc[4 * qd + 3] + Dv * bf_lo(uu[0]);
        u32x2 o = {pk_bf16(y0, y1), pk_bf16(y2, y3)};
        *(u32x2*)(yT + 8 * qd) = o;
    }
}

#define E_(r) "v_exp_f32 v" #r ", v" #r "\n\t"
#define TILE(C0, C1, C2, C3, C4, C5, C6, C7, C8, C9, C10, C11, C12, C13, C14, C15, CLO, NHI, NALL, VH_ADDR, KOFF, VL_ADDR, EXTRA) \
    E_(C0) E_(C1)                                                                                   \
    "s_waitcnt lgkmcnt(2)\n\t"                                                                      \
    "v_smfmac_f32_32x32x32_bf16 v[64:79], v[108:111], " NHI ", v112\n\t"                            \
    "ds_read_b128 v[108:111], " VH_ADDR "\n\t"                                                      \
    E_(C2) E_(C3) E_(C4)                                                                            \
    "s_waitcnt lgkmcnt(2)\n\t"                                                                      \
    "v_mfma_f32_32x32x16_bf16 " NALL ", v[100:103], v[96:99], v[80:95]\n\t"                         \
    "ds_read_b128 v[100:103], v113 offset:" KOFF "\n\t"                                             \
    E_(C5) E_(C6) E_(C7) E_(C8) EXTRA E_(C9) E_(C10)                                                \
    "s_waitcnt lgkmcnt(2)\n\t"                                                                      \
    "v_smfmac_f32_32x32x32_bf16 v[64:79], v[104:107], " CLO ", v112\n\t"                            \
    "ds_read_b128 v[104:107], " VL_ADDR "\n\t"                                                      \
    E_(C11) E_(C12) E_(C13) E_(C14) E_(C15)
#define TILE_A(VH_ADDR, KOFF, VL_ADDR, EXTRA) \
    TILE(32, 33, 34, 35, 36, 37, 38, 39, 40, 41, 42, 43, 44, 45, 46, 47, "v[32:39]", "v[56:63]", "v[48:63]", VH_ADDR, KOFF, VL_ADDR, EXTRA)
#define TILE_B(VH_ADDR, KOFF, VL_ADDR, EXTRA) \
    TILE(48, 49, 50, 51, 52, 53, 54, 55, 56, 57, 58, 59, 60, 61, 62, 63, "v[48:55]", "v[40:47]", "v[32:47]", VH_ADDR, KOFF, VL_ADDR, EXTRA)

__global__ __launch_bounds__(768, 3) void k2_main(const u16* __restrict__ ssm_k, const u16* __restrict__ ssm_D,
                                                  char* __restrict__ ws) {
    extern __shared__ __attribute__((aligned(16))) char smem[];
    const int b = blockIdx.x;
    const int tid = threadIdx.x;
    const int lane = tid & 63;
    const int w = __builtin_amdgcn_readfirstlane(tid >> 6);
    const int hh = lane >> 5;
    const int sb = b >> 3, hg = b & 7;

    u32x4 qf = {0, 0, 0, 0};
    float km = 0.f;
    if (w < 8) {
        const int head = 4 * hg + (w >> 1), st = 2 * sb + (w & 1);
        qf = *(const u32x4*)(ws + WS_QF + ((size_t)(head * 64 + st) * 64 + lane) * 16);
        const f32x2 k2 = *(const f32x2*)(ws + WS_KM + lane * 8);
        km = fmaxf(k2[0], k2[1]);
    }
    u32x4 hn = {0, 0, 0, 0};
    u32 hn4 = 0;
    const int hch = tid >= 260 ? 1 : 0;
    const int hi = tid - 260 * hch;
    if (tid < 520) {
        const u32* nat = (const u32*)(ssm_k + (size_t)(2 * b + hch) * SEQ);
        if (hi >= 4) hn = *(const u32x4*)(nat + 4 * (hi - 4));
        if (hi >= 3 && hi < 259) hn4 = nat[4 * (hi - 4) + 4];
    }
    asm volatile("s_waitcnt vmcnt(0)" : "+v"(qf), "+v"(km), "+v"(hn), "+v"(hn4)::"memory");
    if (w < 8) {
        const char* kg = ws + WS_KF + lane * 16;
        const char* vg = ws + WS_VF + lane * 16;
#pragma unroll
        for (int i = 0; i < 3; ++i) {
            const int c = w * 3 + i;
            const int T = c >> 1;
            if (c & 1) dma16(vg + T * 1024, smem + K2_V + T * 1024);
            else dma16(kg + T * 1024, smem + K2_K + T * 1024);
        }
#pragma unroll
        for (int i = 0; i < 13; ++i) {
            const int c = 24 + i * 8 + w;
            const int T = c >> 1;
            if (c & 1) dma16(vg + T * 1024, smem + K2_V + T * 1024);
            else dma16(kg + T * 1024, smem + K2_K + T * 1024);
        }
    } else {
        const int cw = w - 8, ch = cw & 1;
        const u16* ug = (const u16*)(ws + WS_UR) + (size_t)(2 * b + ch) * SEQ;
        const int i0 = (cw >> 1) ? 3 : 0, i1 = (cw >> 1) ? 5 : 3;
        for (int i = i0; i < i1; ++i) {
            const int z = 64 * i + lane;
            const int P = z / 5;
            int sg = z - 5 * P;
            if (sg == 4) sg = 0;
            const int beta = ch == 0 ? 63 - P : P;
            dma16(ug + 2016 - 32 * beta + 8 * sg, smem + K2_U + (ch == 0 ? 0 : 96 * 80) + i * 1024);
        }
    }
    if (tid < 520) {
        u32* cE = (u32*)(smem + K2_H + hch * 8320) + 4 * hi;
        u32* cO = cE + 1040;
        u32x4 e = {hn[1], hn[2], hn[3], hn4};
        u32x4 o = {__builtin_amdgcn_alignbit(hn[1], hn[0], 16), __builtin_amdgcn_alignbit(hn[2], hn[1], 16),
                   __builtin_amdgcn_alignbit(hn[3], hn[2], 16), __builtin_amdgcn_alignbit(hn4, hn[3], 16)};
        *(u32x4*)cE = e;
        *(u32x4*)cO = o;
    } else if (tid < 520 + 160) {
        *(u32x4*)(smem + K2_U + 64 * 80 + (tid - 520) * 16) = u32x4{0, 0, 0, 0};
    }
    if (tid < 104) *(u32x4*)(smem + K2_ONES + tid * 16) = u32x4{0x3f803f80u, 0x3f803f80u, 0x3f803f80u, 0x3f803f80u};

    if (w >= 8) {
        asm volatile("s_waitcnt vmcnt(0) lgkmcnt(0)\n\ts_barrier\n\ts_barrier" ::: "memory");
        const int cw = w - 8;
        if (cw & 1) conv_wave<1>(smem, cw >> 1, lane, 2 * b + 1, ssm_D, ws);
        else conv_wave<0>(smem, cw >> 1, lane, 2 * b, ssm_D, ws);
        return;
    }

    float q2 = 0.f;
#pragma unroll
    for (int i = 0; i < 4; ++i) q2 += bf_lo(qf[i]) * bf_lo(qf[i]) + bf_hi(qf[i]) * bf_hi(qf[i]);
    q2 += xor_lane_f(q2, 32);
#pragma unroll
    for (int m = 1; m < 64; m <<= 1) km = fmaxf(km, xor_lane_f(km, m));
    float mrow = sqrtf(q2 * km) * 1.002f + 1e-6f;
    int bar_iter = 2;
    asm volatile("s_waitcnt vmcnt(13) lgkmcnt(0)\n\ts_barrier" ::: "memory");
    if (!__all(mrow <= 48.f)) {
        asm volatile("s_waitcnt vmcnt(0)\n\ts_barrier" ::: "memory");
        bar_iter = -1;
        float mx = -3.0e38f;
        for (int T = 0; T < 64; ++T) {
            const bf16x8 kf = as_bf8(lds_rd128(smem + K2_K + T * 1024 + lane * 16));
            f32x16 s = {0};
            s = __builtin_amdgcn_mfma_f32_32x32x16_bf16(kf, as_bf8(qf), s, 0, 0, 0);
#pragma unroll
            for (int i = 0; i < 16; ++i) mx = fmaxf(mx, s[i]);
        }
        mrow = fmaxf(mx, xor_lane_f(mx, 32));
    }
    bar_iter = __builtin_amdgcn_readfirstlane(bar_iter);
    const float negm = -mrow;
    const int row = lane & 31;
    const bool ones = (row == 16) || (row == 20);
    const u32 lds0 = (u32)(size_t)(lds_void_p)smem;
    u32 kaddr = lds0 + K2_K + lane * 16;
    u32 va0 = ones ? (lds0 + K2_ONES + (row == 20 ? 64 : 0)) : (lds0 + K2_V + hh * 256 + (row & 15) * 16);
    const u32 vinc = ones ? 0u : 4096u;
    u32 va1 = ones ? va0 : va0 + 2048;
    f32x16 O;
    asm volatile(
        "v_mov_b32 v80, %[negm]\n\t v_mov_b32 v81, %[negm]\n\t v_mov_b32 v82, %[negm]\n\t v_mov_b32 v83, %[negm]\n\t"
        "v_mov_b32 v84, %[negm]\n\t v_mov_b32 v85, %[negm]\n\t v_mov_b32 v86, %[negm]\n\t v_mov_b32 v87, %[negm]\n\t"
        "v_mov_b32 v88, %[negm]\n\t v_mov_b32 v89, %[negm]\n\t v_mov_b32 v90, %[negm]\n\t v_mov_b32 v91, %[negm]\n\t"
        "v_mov_b32 v92, %[negm]\n\t v_mov_b32 v93, %[negm]\n\t v_mov_b32 v94, %[negm]\n\t v_mov_b32 v95, %[negm]\n\t"
        "ds_read_b128 v[100:103], v113\n\t"
        "v_mov_b32 v112, 0xdddd\n\t"
        "v_mov_b32 v64, 0\n\t v_mov_b32 v65, 0\n\t v_mov_b32 v66, 0\n\t v_mov_b32 v67, 0\n\t"
        "v_mov_b32 v68, 0\n\t v_mov_b32 v69, 0\n\t v_mov_b32 v70, 0\n\t v_mov_b32 v71, 0\n\t"
        "v_mov_b32 v72, 0\n\t v_mov_b32 v73, 0\n\t v_mov_b32 v74, 0\n\t v_mov_b32 v75, 0\n\t"
        "v_mov_b32 v76, 0\n\t v_mov_b32 v77, 0\n\t v_mov_b32 v78, 0\n\t v_mov_b32 v79, 0\n\t"
        "v_mov_b32 v56, 0\n\t v_mov_b32 v57, 0\n\t v_mov_b32 v58, 0\n\t v_mov_b32 v59, 0\n\t"
        "v_mov_b32 v60, 0\n\t v_mov_b32 v61, 0\n\t v_mov_b32 v62, 0\n\t v_mov_b32 v63, 0\n\t"
        "v_mov_b32 v108, 0\n\t v_mov_b32 v109, 0\n\t v_mov_b32 v110, 0\n\t v_mov_b32 v111, 0\n\t"
        "s_waitcnt lgkmcnt(0)\n\t"
        "v_mfma_f32_32x32x16_bf16 v[32:47], v[100:103], v[96:99], v[80:95]\n\t"
        "s_nop 7\n\t"
        "ds_read_b128 v[100:103], v113 offset:1024\n\t"
        "ds_read_b128 v[104:107], v114\n\t"
        "s_mov_b32 s40, 0\n\t"
        "s_nop 7\n\t"
        "s_waitcnt lgkmcnt(0)\n\t"
        "L_loop_%=:\n\t"
        "s_cmp_lg_u32 s40, %[bar]\n\t"
        "s_cbranch_scc1 L_nobar_%=\n\t"
        "s_waitcnt vmcnt(0)\n\t"
        "s_barrier\n\t"
        "L_nobar_%=:\n\t"
        TILE_A("v114 offset:512", "2048", "v114 offset:1024", "")
        TILE_B("v114 offset:1536", "3072", "v115", "")
        TILE_A("v115 offset:512", "4096", "v115 offset:1024", "v_add_u32 v114, v114, v116\n\t")
        TILE_B("v115 offset:1536", "5120", "v114", "")
        "v_add_u32 v113, 0x1000, v113\n\t"
        "v_add_u32 v115, v115, v116\n\t"
        "s_add_u32 s40, s40, 1\n\t"
        "s_cmp_lt_u32 s40, 16\n\t"
        "s_cbranch_scc1 L_loop_%=\n\t"
        "s_waitcnt lgkmcnt(0)\n\t"
        "v_smfmac_f32_32x32x32_bf16 v[64:79], v[108:111], v[56:63], v112\n\t"
        "s_nop 15\n\t"
        "s_nop 7\n\t"
        : "=&{v[64:79]}"(O), "+{v113}"(kaddr), "+{v114}"(va0), "+{v115}"(va1)
        : [negm] "v"(negm), [bar] "s"(bar_iter), "{v[96:99]}"(qf), "{v116}"(vinc)
        : "memory", "scc", "s40", "v32", "v33", "v34", "v35", "v36", "v37", "v38", "v39", "v40", "v41", "v42", "v43", "v44", "v45",
          "v46", "v47", "v48", "v49", "v50", "v51", "v52", "v53", "v54", "v55", "v56", "v57", "v58", "v59", "v60", "v61",
          "v62", "v63", "v80", "v81", "v82", "v83", "v84", "v85", "v86", "v87", "v88", "v89", "v90", "v91", "v92", "v93",
          "v94", "v95", "v100", "v101", "v102", "v103", "v104", "v105", "v106", "v107", "v108", "v109", "v110", "v111", "v112");
    {
        const float inv = 1.f / O[8];
        u32x4 o = {pk_bf16(O[0] * inv, O[1] * inv), pk_bf16(O[2] * inv, O[3] * inv), pk_bf16(O[4] * inv, O[5] * inv),
                   pk_bf16(O[6] * inv, O[7] * inv)};
        const int head = 4 * hg + (w >> 1), st = 2 * sb + (w & 1);
        *(u32x4*)(ws + WS_OF + ((size_t)(head * 64 + st) * 64 + lane) * 16) = o;
    }
}

constexpr int K3_O = 0;
constexpr int K3_W = 65536;
constexpr int K3_EX = 131072;
constexpr int K3_LDS = 147456;

__global__ __launch_bounds__(512) void k3_out(const u16* __restrict__ x, const u16* __restrict__ wo,
                                              const char* __restrict__ ws, u16* __restrict__ out) {
    extern __shared__ __attribute__((aligned(16))) char smem[];
    const int b = blockIdx.x;
    const int sb = (b & 7) * 4 + (b >> 6);
    const int nb = (b >> 3) & 7;
    const int tid = threadIdx.x;
    const int lane = tid & 63;
    const int w = __builtin_amdgcn_readfirstlane(tid >> 6);
    const int hh = lane >> 5;
    const int nt = w & 1, st = (w >> 1) & 1, kh = w >> 2;
    const int r31 = lane & 31;

    const int s = 64 * sb + 32 * st + r31;
    const int n0 = 64 * nb + 32 * nt + 16 * kh + 4 * hh;
    const u32x2 xa = *(const u32x2*)(x + (size_t)s * DM + n0);
    const u32x2 xb2 = *(const u32x2*)(x + (size_t)s * DM + n0 + 8);
    const u16* yT = (const u16*)(ws + WS_YT);
    u16 yv[8];
#pragma unroll
    for (int i = 0; i < 8; ++i) yv[i] = yT[(size_t)(n0 + (i & 3) + 8 * (i >> 2)) * SEQ + s];
    const float g = ((const float*)(ws + WS_G))[s];

#pragma unroll
    for (int i = 0; i < 8; ++i) {
        const int c = w * 8 + i;
        dma16(ws + WS_OF + ((size_t)((c >> 1) * 64 + 2 * sb + (c & 1)) * 64 + lane) * 16, smem + K3_O + c * 1024);
    }
    const u16* wg = wo + nb * 64;
#pragma unroll
    for (int i = 0; i < 8; ++i) {
        const int k0 = (w * 8 + i) * 8;
        const int k = k0 + (lane >> 3);
        const int cp = lane & 7;
        dma16(wg + k * DM + 8 * (cp ^ (4 * ((k >> 1) & 1))), smem + K3_W + k0 * 128);
    }
    asm volatile("s_waitcnt vmcnt(0)" ::: "memory");
    __syncthreads();

    const int blk = (lane >> 4) & 1, q = (lane & 15) >> 2, p = lane & 3;
    const char* ob = smem + K3_O + (kh * 32 + st) * 1024 + lane * 16;
    const char* wb = smem + K3_W + (kh * 256 + 4 * hh + q) * 128 + 64 * (nt ^ ((q >> 1) & 1)) + 32 * blk + 8 * p;
    f32x16 acc = {0};
#pragma unroll
    for (int i = 0; i < 16; ++i) {
        const bf16x8 of = as_bf8(lds_rd128(ob + i * 2048));
        const bf16x8 wf = cat_tr(lds_tr(wb + i * 2048), lds_tr(wb + i * 2048 + 1024));
        acc = __builtin_amdgcn_mfma_f32_32x32x16_bf16(wf, of, acc, 0, 0, 0);
    }
    char* ex = smem + K3_EX;
    f32x4 s0, s1, k0, k1;
    if (kh == 0) {
        s0 = f32x4{acc[8], acc[9], acc[10], acc[11]};
        s1 = f32x4{acc[12], acc[13], acc[14], acc[15]};
        k0 = f32x4{acc[0], acc[1], acc[2], acc[3]};
        k1 = f32x4{acc[4], acc[5], acc[6], acc[7]};
    } else {
        s0 = f32x4{acc[0], acc[1], acc[2], acc[3]};
        s1 = f32x4{acc[4], acc[5], acc[6], acc[7]};
        k0 = f32x4{acc[8], acc[9], acc[10], acc[11]};
        k1 = f32x4{acc[12], acc[13], acc[14], acc[15]};
    }
    *(f32x4*)(ex + w * 2048 + lane * 16) = s0;
    *(f32x4*)(ex + w * 2048 + 1024 + lane * 16) = s1;
    __syncthreads();
    k0 += *(const f32x4*)(ex + (w ^ 4) * 2048 + lane * 16);
    k1 += *(const f32x4*)(ex + (w ^ 4) * 2048 + 1024 + lane * 16);
    const float g1 = 1.f - g;
    float o[8];
    o[0] = g * bf_lo(xa[0]) * bf_lo((u32)yv[0]) + g1 * k0[0];
    o[1] = g * bf_hi(xa[0]) * bf_lo((u32)yv[1]) + g1 * k0[1];
    o[2] = g * bf_lo(xa[1]) * bf_lo((u32)yv[2]) + g1 * k0[2];
    o[3] = g * bf_hi(xa[1]) * bf_lo((u32)yv[3]) + g1 * k0[3];
    o[4] = g * bf_lo(xb2[0]) * bf_lo((u32)yv[4]) + g1 * k1[0];
    o[5] = g * bf_hi(xb2[0]) * bf_lo((u32)yv[5]) + g1 * k1[1];
    o[6] = g * bf_lo(xb2[1]) * bf_lo((u32)yv[6]) + g1 * k1[2];
    o[7] = g * bf_hi(xb2[1]) * bf_lo((u32)yv[7]) + g1 * k1[3];
    u32 a0 = pk_bf16(o[0], o[1]), a1 = pk_bf16(o[2], o[3]), b0 = pk_bf16(o[4], o[5]), b1 = pk_bf16(o[6], o[7]);
    auto r0 = __builtin_amdgcn_permlane32_swap(a0, b0, false, false);
    auto r1 = __builtin_amdgcn_permlane32_swap(a1, b1, false, false);
    u32x4 ov = {r0[0], r1[0], r0[1], r1[1]};
    *(u32x4*)(out + (size_t)s * DM + 64 * nb + 32 * nt + 16 * kh + 8 * hh) = ov;
}

extern "C" void kernel_launch(void* const* d_in, const int* in_sizes, int n_in, void* d_out, int out_size, void* d_ws,
                              size_t ws_size, hipStream_t stream) {
    const u16* A_q = (const u16*)d_in[0];
    const u16* A_k = (const u16*)d_in[1];
    const u16* A_v = (const u16*)d_in[2];
    const u16* shift_k = (const u16*)d_in[3];
    const u16* ssm_k = (const u16*)d_in[4];
    const u16* ssm_D = (const u16*)d_in[5];
    const u16* wq = (const u16*)d_in[7];
    const u16* wk = (const u16*)d_in[8];
    const u16* wv = (const u16*)d_in[9];
    const u16* wo = (const u16*)d_in[10];
    const u16* gate_w = (const u16*)d_in[11];
    char* ws = (char*)d_ws;
    (void)hipFuncSetAttribute((const void*)k1_prep, hipFuncAttributeMaxDynamicSharedMemorySize, K1_LDS);
    (void)hipFuncSetAttribute((const void*)k2_main, hipFuncAttributeMaxDynamicSharedMemorySize, K2_LDS);
    (void)hipFuncSetAttribute((const void*)k3_out, hipFuncAttributeMaxDynamicSharedMemorySize, K3_LDS);
    k1_prep<<<256, 512, K1_LDS, stream>>>(A_q, A_k, A_v, shift_k, wq, wk, wv, gate_w, ws);
    k2_main<<<256, 768, K2_LDS, stream>>>(ssm_k, ssm_D, ws);
    k3_out<<<256, 512, K3_LDS, stream>>>(A_q, wo, ws, (u16*)d_out);
}
```

```cpp
#include <hip/hip_runtime.h>
#include <hip/hip_bf16.h>
#include <stdint.h>

typedef unsigned short u16;
typedef unsigned int u32;
typedef __attribute__((ext_vector_type(8))) __bf16 bf16x8;
typedef __attribute__((ext_vector_type(4))) short s16x4;
typedef __attribute__((ext_vector_type(16))) float f32x16;
typedef __attribute__((ext_vector_type(4))) float f32x4;
typedef __attribute__((ext_vector_type(4))) u32 u32x4;
typedef __attribute__((ext_vector_type(2))) u32 u32x2;
typedef __attribute__((ext_vector_type(2))) float f32x2;
typedef __attribute__((ext_vector_type(2))) __bf16 bf16x2;
typedef __attribute__((address_space(3))) s16x4* lds_s16x4_p;
typedef __attribute__((address_space(3))) void* lds_void_p;
typedef const __attribute__((address_space(1))) void* glb_cvoid_p;

#define DEV __device__ __forceinline__
#define PREFETCH 1
#define STAMP_DECL
#define STAMP(kid, idx)
#define STAMPR(kid, idx)
#define STAMP_FLUSH(kid)

constexpr int SEQ = 2048;
constexpr int DM = 512;

constexpr size_t WS_QF = 0;
constexpr size_t WS_OF = WS_QF + 2097152;
constexpr size_t WS_UR = WS_OF + 2097152;
constexpr size_t WS_YT = WS_UR + 2097152;
constexpr size_t WS_KF = WS_YT + 2097152;
constexpr size_t WS_VF = WS_KF + 65536;
constexpr size_t WS_G = WS_VF + 65536;
constexpr size_t WS_KM = WS_G + 8192;

DEV float bf_lo(u32 v) { return __builtin_bit_cast(float, v << 16); }
DEV float bf_hi(u32 v) { return __builtin_bit_cast(float, v & 0xffff0000u); }
DEV u32 pk_bf16(float a, float b) {
    f32x2 f = {a, b};
    bf16x2 r = __builtin_convertvector(f, bf16x2);
    return __builtin_bit_cast(u32, r);
}
DEV void dma16(const void* g, char* lds) {
    __builtin_amdgcn_global_load_lds((glb_cvoid_p)g, (lds_void_p)lds, 16, 0, 0);
}
DEV bf16x8 as_bf8(u32x4 v) { return __builtin_bit_cast(bf16x8, v); }
DEV u32x4 lds_rd128(const char* p) { return *(const u32x4*)p; }
DEV u32x2 lds_tr(const char* p) {
    s16x4 v = __builtin_amdgcn_ds_read_tr16_b64_v4i16((lds_s16x4_p)(p));
    return __builtin_bit_cast(u32x2, v);
}
DEV bf16x8 cat_tr(u32x2 a, u32x2 b) {
    u32x4 r = {a[0], a[1], b[0], b[1]};
    return as_bf8(r);
}
DEV float xor_lane_f(float v, int m) { return __shfl_xor(v, m, 64); }
template <int CTRL>
DEV float dpp_mov(float old, float v) {
    return __builtin_bit_cast(float, __builtin_amdgcn_update_dpp(__builtin_bit_cast(int, old), __builtin_bit_cast(int, v), CTRL, 0xf, 0xf, false));
}
DEV float wave_max(float v) {
    v = fmaxf(v, dpp_mov<0x111>(v, v));
    v = fmaxf(v, dpp_mov<0x112>(v, v));
    v = fmaxf(v, dpp_mov<0x114>(v, v));
    v = fmaxf(v, dpp_mov<0x118>(v, v));
    v = fmaxf(v, dpp_mov<0x142>(v, v));
    v = fmaxf(v, dpp_mov<0x143>(v, v));
    return __builtin_bit_cast(float, __builtin_amdgcn_readlane(__builtin_bit_cast(int, v), 63));
}
DEV float wave_sum(float v) {
    v += dpp_mov<0x111>(0.f, v);
    v += dpp_mov<0x112>(0.f, v);
    v += dpp_mov<0x114>(0.f, v);
    v += dpp_mov<0x118>(0.f, v);
    v += dpp_mov<0x142>(0.f, v);
    v += dpp_mov<0x143>(0.f, v);
    return __builtin_bit_cast(float, __builtin_amdgcn_readlane(__builtin_bit_cast(int, v), 63));
}
DEV float add_halves(float v) {
    const u32 x = __builtin_bit_cast(u32, v);
    auto r = __builtin_amdgcn_permlane32_swap(x, x, false, false);
    return __builtin_bit_cast(float, (u32)r[0]) + __builtin_bit_cast(float, (u32)r[1]);
}
DEV float max_halves(float v) {
    const u32 x = __builtin_bit_cast(u32, v);
    auto r = __builtin_amdgcn_permlane32_swap(x, x, false, false);
    return fmaxf(__builtin_bit_cast(float, (u32)r[0]), __builtin_bit_cast(float, (u32)r[1]));
}

constexpr int K1_X = 0;
constexpr int K1_W = 65536;
constexpr int K1_WKV = 131072;
constexpr int K1_EX = 147456;
constexpr int K1_LDS = 163840;

__global__ __launch_bounds__(512) void k1_prep(const u16* __restrict__ x, const u16* __restrict__ kin,
                                               const u16* __restrict__ vin, const u16* __restrict__ shift_k,
                                               const u16* __restrict__ wq, const u16* __restrict__ wk,
                                               const u16* __restrict__ wv, const u16* __restrict__ gate_w,
                                               char* __restrict__ ws) {
    extern __shared__ __attribute__((aligned(16))) char smem[];
    const int b = blockIdx.x;
    const int sb = (b & 7) * 4 + (b >> 6);
    const int nb = (b >> 3) & 7;
    const int tid = threadIdx.x;
    const int lane = tid & 63;
    const int w = __builtin_amdgcn_readfirstlane(tid >> 6);
    const int hh = lane >> 5;

    STAMP_DECL STAMPR(0, 14); STAMP(0, 0);
    u32x4 gw = {0, 0, 0, 0};
    if (w >= 4) {
        const int wl = w - 4;
        const u16* xg = x + (size_t)(64 * sb) * DM;
#pragma unroll
        for (int i = 0; i < 16; ++i) {
            const int row = wl * 16 + i;
            dma16(xg + row * DM + 8 * (lane ^ (row & 15)), smem + K1_X + row * 1024);
        }
        const u16* wg = wq + nb * 64;
#pragma unroll
        for (int i = 0; i < 16; ++i) {
            const int k0 = (wl * 16 + i) * 8;
            const int k = k0 + (lane >> 3);
            const int cp = lane & 7;
            dma16(wg + k * DM + 8 * (cp ^ (4 * ((k >> 1) & 1))), smem + K1_W + k0 * 128);
        }
        const u16* wkv = (nb & 1) ? wv : wk;
#pragma unroll
        for (int i = 0; i < 4; ++i) {
            const int c = wl * 4 + i;
            dma16(wkv + c * 512 + lane * 8, smem + K1_WKV + c * 1024);
        }
    } else {
        const int tb = lane & 7, dp = lane >> 3;
        const int d0 = 64 * nb + 16 * w + 2 * dp;
        const int t0 = 64 * sb + 8 * tb;
        u32 kr[11], vr[8];
#pragma unroll
        for (int i = 0; i < 11; ++i) {
            const int t = t0 - 3 + i;
            kr[i] = (t >= 0) ? *(const u32*)(kin + (size_t)t * DM + d0) : 0u;
        }
#pragma unroll
        for (int i = 0; i < 8; ++i) vr[i] = *(const u32*)(vin + (size_t)(t0 + i) * DM + d0);
        const u32x4 sw = *(const u32x4*)(shift_k + d0 * 4);
        const float w00 = bf_lo(sw[0]), w01 = bf_hi(sw[0]), w02 = bf_lo(sw[1]), w03 = bf_hi(sw[1]);
        const float w10 = bf_lo(sw[2]), w11 = bf_hi(sw[2]), w12 = bf_lo(sw[3]), w13 = bf_hi(sw[3]);
        float u0[8], u1[8];
#pragma unroll
        for (int i = 0; i < 8; ++i) {
            const float a = bf_lo(kr[i + 3]) * w00 + bf_lo(kr[i + 2]) * w01 + bf_lo(kr[i + 1]) * w02 + bf_lo(kr[i]) * w03;
            const float c = bf_hi(kr[i + 3]) * w10 + bf_hi(kr[i + 2]) * w11 + bf_hi(kr[i + 1]) * w12 + bf_hi(kr[i]) * w13;
            u0[i] = a * bf_lo(vr[i]);
            u1[i] = c * bf_hi(vr[i]);
        }
        u16* uR = (u16*)(ws + WS_UR);
        const int i0 = 2040 - t0;
        u32x4 o0 = {pk_bf16(u0[7], u0[6]), pk_bf16(u0[5], u0[4]), pk_bf16(u0[3], u0[2]), pk_bf16(u0[1], u0[0])};
        u32x4 o1 = {pk_bf16(u1[7], u1[6]), pk_bf16(u1[5], u1[4]), pk_bf16(u1[3], u1[2]), pk_bf16(u1[1], u1[0])};
        *(u32x4*)(uR + (size_t)d0 * SEQ + i0) = o0;
        *(u32x4*)(uR + (size_t)(d0 + 1) * SEQ + i0) = o1;
    }
    STAMP(0, 1);
    gw = *(const u32x4*)(gate_w + lane * 8);
    asm volatile("s_waitcnt vmcnt(0)" ::: "memory");
    STAMP(0, 2);
    __syncthreads();
    STAMP(0, 3);

    {
        const int row = 8 * nb + w;
        const u32x4 xv = lds_rd128(smem + K1_X + row * 1024 + 16 * (lane ^ (row & 15)));
        float s = 0.f;
#pragma unroll
        for (int i = 0; i < 4; ++i) s += bf_lo(xv[i]) * bf_lo(gw[i]) + bf_hi(xv[i]) * bf_hi(gw[i]);
#pragma unroll
        for (int m = 1; m < 64; m <<= 1) s += xor_lane_f(s, m);
        if (lane == 0) ((float*)(ws + WS_G))[64 * sb + row] = 1.f / (1.f + __expf(-s));
    }

    if (w == 7) {
        const int kg = nb >> 1;
        const int g = lane >> 4, c16 = lane & 15;
        const int row = 16 * kg + c16;
        const int q = c16 >> 2, p = lane & 3;
        f32x4 acc = {0.f, 0.f, 0.f, 0.f};
        const char* xb = smem + K1_X + row * 1024;
        const char* wb = smem + K1_WKV + (8 * g + q) * 32 + 8 * p;
        if (nb & 1) {
#pragma unroll
            for (int i = 0; i < 16; ++i) {
                const bf16x8 xf = as_bf8(lds_rd128(xb + 16 * ((4 * i + g) ^ c16)));
                const bf16x8 wf = cat_tr(lds_tr(wb + i * 1024), lds_tr(wb + i * 1024 + 128));
                acc = __builtin_amdgcn_mfma_f32_16x16x32_bf16(xf, wf, acc, 0, 0, 0);
            }
            const int T = 2 * sb + (kg >> 1), s = kg & 1;
            u32x2 o = {pk_bf16(acc[0], acc[1]), pk_bf16(acc[2], acc[3])};
            char* dst = ws + WS_VF + (size_t)(T * 2 + s) * 512 + (g & 1) * 256 + c16 * 16 + 8 * (g >> 1);
            *(u32x2*)dst = o;
        } else {
#pragma unroll
            for (int i = 0; i < 16; ++i) {
                const bf16x8 xf = as_bf8(lds_rd128(xb + 16 * ((4 * i + g) ^ c16)));
                const bf16x8 wf = cat_tr(lds_tr(wb + i * 1024), lds_tr(wb + i * 1024 + 128));
                acc = __builtin_amdgcn_mfma_f32_16x16x32_bf16(wf, xf, acc, 0, 0, 0);
            }
            const int T = 2 * sb + (kg >> 1);
            const int key = 16 * (kg & 1) + c16;
            const int rho = (key & 19) | ((key & 4) << 1) | ((key & 8) >> 1);
            u32x2 o = {pk_bf16(acc[0], acc[1]), pk_bf16(acc[2], acc[3])};
            char* dst = ws + WS_KF + (size_t)T * 1024 + (rho + 32 * (g & 1)) * 16 + 8 * (g >> 1);
            *(u32x2*)dst = o;
            float n2 = bf_lo(o[0]) * bf_lo(o[0]) + bf_hi(o[0]) * bf_hi(o[0]) + bf_lo(o[1]) * bf_lo(o[1]) + bf_hi(o[1]) * bf_hi(o[1]);
            n2 += xor_lane_f(n2, 16);
            n2 += xor_lane_f(n2, 32);
#pragma unroll
            for (int m = 1; m < 16; m <<= 1) n2 = fmaxf(n2, xor_lane_f(n2, m));
            if (lane == 0) ((float*)(ws + WS_KM))[4 * sb + kg] = n2;
        }
    }

    STAMP(0, 4);
    {
        const int nt = w & 1, st = (w >> 1) & 1, kh = w >> 2;
        const int r31 = lane & 31;
        const int row = 32 * st + r31;
        const int blk = (lane >> 4) & 1, q = (lane & 15) >> 2, p = lane & 3;
        const char* xb = smem + K1_X + row * 1024 + kh * 512;
        const char* wb = smem + K1_W + (kh * 256 + 8 * hh + q) * 128 + 64 * (nt ^ ((q >> 1) & 1)) + 32 * blk + 8 * p;
        f32x16 acc = {0};
#pragma unroll
        for (int i = 0; i < 16; ++i) {
            const int c = 2 * i + hh;
            const bf16x8 xf = as_bf8(lds_rd128(xb + 16 * ((c & 16) | ((c ^ row) & 15))));
            const bf16x8 wf = cat_tr(lds_tr(wb + i * 2048), lds_tr(wb + i * 2048 + 512));
            acc = __builtin_amdgcn_mfma_f32_32x32x16_bf16(wf, xf, acc, 0, 0, 0);
        }
        char* ex = smem + K1_EX;
        f32x4 s0, s1, k0, k1;
        if (kh == 0) {
            s0 = f32x4{acc[8], acc[9], acc[10], acc[11]};
            s1 = f32x4{acc[12], acc[13], acc[14], acc[15]};
            k0 = f32x4{acc[0], acc[1], acc[2], acc[3]};
            k1 = f32x4{acc[4], acc[5], acc[6], acc[7]};
        } else {
            s0 = f32x4{acc[0], acc[1], acc[2], acc[3]};
            s1 = f32x4{acc[4], acc[5], acc[6], acc[7]};
            k0 = f32x4{acc[8], acc[9], acc[10], acc[11]};
            k1 = f32x4{acc[12], acc[13], acc[14], acc[15]};
        }
        *(f32x4*)(ex + w * 2048 + lane * 16) = s0;
        *(f32x4*)(ex + w * 2048 + 1024 + lane * 16) = s1;
        __syncthreads();
        const f32x4 r0 = *(const f32x4*)(ex + (w ^ 4) * 2048 + lane * 16);
        const f32x4 r1 = *(const f32x4*)(ex + (w ^ 4) * 2048 + 1024 + lane * 16);
        const float sc = 0.25f * 1.4426950408889634f;
        k0 = (k0 + r0) * sc;
        k1 = (k1 + r1) * sc;
        const int head = 4 * nb + 2 * nt + kh;
        u32x4 o = {pk_bf16(k0[0], k0[1]), pk_bf16(k0[2], k0[3]), pk_bf16(k1[0], k1[1]), pk_bf16(k1[2], k1[3])};
        *(u32x4*)(ws + WS_QF + ((size_t)(head * 64 + 2 * sb + st) * 64 + lane) * 16) = o;
    }
    STAMP(0, 5); STAMPR(0, 15); STAMP_FLUSH(0);
}

constexpr int K2_K = 0;
constexpr int K2_V = 65536;
constexpr int K2_ONES = 131072;
constexpr int K2_H = K2_ONES + 1664;
constexpr int K2_U = K2_H + 4 * 4160;
constexpr int K2_FLAG = K2_U + 160 * 80;
constexpr int K2_LDS = K2_FLAG + 16;
static_assert(K2_LDS <= 163840, "lds");
static_assert((K2_U & 15) == 0 && (K2_H & 15) == 0, "align");

#define CV_RD(SET_A0, SET_A1, SET_B, MI, UOFF)                                                    \
    "ds_read2_b32 " SET_A0 ", v64 offset0:" #MI "*8 offset1:" #MI "*8+1\n\t"                      \
    "ds_read2_b32 " SET_A1 ", v64 offset0:" #MI "*8+2 offset1:" #MI "*8+3\n\t"                    \
    "ds_read_b128 " SET_B ", v65 offset:" UOFF "\n\t"
#define CV_RD0(MI, UOFF) CV_RD("v[32:33]", "v[34:35]", "v[36:39]", MI, UOFF)
#define CV_RD1(MI, UOFF) CV_RD("v[40:41]", "v[42:43]", "v[44:47]", MI, UOFF)
#define CV_RD2(MI, UOFF) CV_RD("v[48:49]", "v[50:51]", "v[52:55]", MI, UOFF)
#define CV_RD3(MI, UOFF) CV_RD("v[56:57]", "v[58:59]", "v[60:63]", MI, UOFF)
#define CV_MM(A, B) "s_waitcnt lgkmcnt(9)\n\tv_mfma_f32_32x32x16_bf16 v[16:31], " A ", " B ", v[16:31]\n\t" CV_PACE
#define CV_MM0 CV_MM("v[32:35]", "v[36:39]")
#define CV_MM1 CV_MM("v[40:43]", "v[44:47]")
#define CV_MM2 CV_MM("v[48:51]", "v[52:55]")
#define CV_MM3 CV_MM("v[56:59]", "v[60:63]")
#define CONV_SLEEP 0
#define CONV_PRIO 0
#define CV_STR2(x) #x
#define CV_STR(x) CV_STR2(x)
#define CV_PACE ""
#define CV_BODY(UO)                                                        \
    CV_RD3(2, UO(1) "+0")  CV_MM0            \
    CV_RD0(5, UO(2) "+32") CV_MM1                               \
    CV_RD1(4, UO(2) "+0")  CV_MM2                                           \
    CV_RD2(7, UO(3) "+32") CV_MM3                                           \
    CV_RD3(6, UO(3) "+0")  CV_MM0                                           \
    CV_RD0(9, UO(4) "+32") CV_MM1                                           \
    CV_RD1(8, UO(4) "+0")  CV_MM2                                           \
    CV_RD2(11, UO(5) "+32") CV_MM3                                          \
    CV_RD3(10, UO(5) "+0") CV_MM0                                           \
    CV_RD0(13, UO(6) "+32") CV_MM1                                          \
    CV_RD1(12, UO(6) "+0") CV_MM2                                           \
    CV_RD2(15, UO(7) "+32") CV_MM3                                          \
    CV_RD3(14, UO(7) "+0") CV_MM0                                           \
    CV_RD0(17, UO(8) "+32") CV_MM1                                          \
    CV_RD1(16, UO(8) "+0") CV_MM2                                           \
    CV_RD2(19, UO(9) "+32") CV_MM3
#define CV_UO0(dd) "80*" #dd
#define CV_UO1(dd) "80*(9-" #dd ")"
#define CV_ASM(UO, USTEP)                                                                          \
    asm volatile(                                                                                  \
        "s_setprio " CV_STR(CONV_PRIO) "\n\t"                                                                          \
        "v_mov_b32 v16, 0\n\t v_mov_b32 v17, 0\n\t v_mov_b32 v18, 0\n\t v_mov_b32 v19, 0\n\t"     \
        "v_mov_b32 v20, 0\n\t v_mov_b32 v21, 0\n\t v_mov_b32 v22, 0\n\t v_mov_b32 v23, 0\n\t"     \
        "v_mov_b32 v24, 0\n\t v_mov_b32 v25, 0\n\t v_mov_b32 v26, 0\n\t v_mov_b32 v27, 0\n\t"     \
        "v_mov_b32 v28, 0\n\t v_mov_b32 v29, 0\n\t v_mov_b32 v30, 0\n\t v_mov_b32 v31, 0\n\t"     \
        CV_RD0(1, UO(0) "+32") CV_RD1(0, UO(0) "+0") CV_RD2(3, UO(1) "+32")                        \
        "s_mov_b32 s41, %[n]\n\t"                                                                  \
        "L_cv_%=:\n\t"                                                                             \
        CV_BODY(UO)                                                                                \
        "v_add_u32 v64, 0x200, v64\n\t"                                                            \
        "v_add_u32 v65, " USTEP ", v65\n\t"                                                        \
        "s_sub_u32 s41, s41, 1\n\t"                                                                \
        "s_cmp_lg_u32 s41, 0\n\t"                                                                  \
        "s_cbranch_scc1 L_cv_%=\n\t"                                                               \
        "s_waitcnt lgkmcnt(0)\n\t"                                                                 \
        "s_nop 15\n\t"                                                                             \
        : "=&{v[16:31]}"(acc), "+{v64}"(ha), "+{v65}"(ua)                                          \
        : [n] "s"(nit)                                                                             \
        : "memory", "scc", "s41", "v32", "v33", "v34", "v35", "v36", "v37", "v38", "v39", "v40", "v41", "v42", "v43", \
          "v44", "v45", "v46", "v47", "v48", "v49", "v50", "v51", "v52", "v53", "v54", "v55", "v56", "v57", "v58", "v59", \
          "v60", "v61", "v62", "v63")

template <int CH>
DEV void conv_wave(char* smem, int nt, int lane, int ch_global, const u16* __restrict__ ssm_D, char* __restrict__ ws) {
    const int r = lane & 31, hh = lane >> 5;
    const u32 lds0 = (u32)(size_t)(lds_void_p)smem;
    u32 ha = lds0 + K2_H + CH * 8320 + ((r & 1) ? 0 : 4160) + 16 * hh + 4 * (r >> 1);
    u32 ua;
    if (CH == 0) ua = lds0 + K2_U + (63 - 32 * nt - r) * 80 + 16 * hh;
    else ua = lds0 + K2_U + 96 * 80 + (32 * nt + r - 9) * 80 + 16 * hh;
    const float Dv = bf_lo((u32)ssm_D[ch_global]);
    const int nit = __builtin_amdgcn_readfirstlane(4 * (nt + 1));
    f32x16 acc;
    if (CH == 0) CV_ASM(CV_UO0, "0x280");
    else CV_ASM(CV_UO1, "0xfffffd80");
    const int beta = 32 * nt + r;
    const char* ublk = (CH == 0) ? (smem + K2_U + (63 - beta) * 80) : (smem + K2_U + 96 * 80 + beta * 80);
    u16* yT = (u16*)(ws + WS_YT) + (size_t)ch_global * SEQ + 32 * beta + 4 * hh;
#pragma unroll
    for (int qd = 0; qd < 4; ++qd) {
        const u32x2 uu = *(const u32x2*)(ublk + 2 * (28 - 8 * qd - 4 * hh));
        const float y0 = acc[4 * qd + 0] + Dv * bf_hi(uu[1]);
        const float y1 = acc[4 * qd + 1] + Dv * bf_lo(uu[1]);
        const float y2 = acc[4 * qd + 2] + Dv * bf_hi(uu[0]);
        const float y3 = acc[4 * qd + 3] + Dv * bf_lo(uu[0]);
        u32x2 o = {pk_bf16(y0, y1), pk_bf16(y2, y3)};
        *(u32x2*)(yT + 8 * qd) = o;
    }
}

#define E_(r) "v_exp_f32 v" #r ", v" #r "\n\t"
#define TILE(C0, C1, C2, C3, C4, C5, C6, C7, C8, C9, C10, C11, C12, C13, C14, C15, CLO, NHI, NALL, VH_ADDR, KOFF, VL_ADDR, EXTRA) \
    E_(C0) E_(C1)                                                                                   \
    "s_waitcnt lgkmcnt(2)\n\t"                                                                      \
    "v_smfmac_f32_32x32x32_bf16 v[64:79], v[108:111], " NHI ", v112\n\t"                            \
    "ds_read_b128 v[108:111], " VH_ADDR "\n\t"                                                      \
    E_(C2) E_(C3) E_(C4)                                                                            \
    "s_waitcnt lgkmcnt(2)\n\t"                                                                      \
    "v_mfma_f32_32x32x16_bf16 " NALL ", v[100:103], v[96:99], v[80:95]\n\t"                         \
    "ds_read_b128 v[100:103], v113 offset:" KOFF "\n\t"                                             \
    E_(C5) E_(C6) E_(C7) E_(C8) EXTRA E_(C9) E_(C10)                                                \
    "s_waitcnt lgkmcnt(2)\n\t"                                                                      \
    "v_smfmac_f32_32x32x32_bf16 v[64:79], v[104:107], " CLO ", v112\n\t"                            \
    "ds_read_b128 v[104:107], " VL_ADDR "\n\t"                                                      \
    E_(C11) E_(C12) E_(C13) E_(C14) E_(C15)
#define PRIO_MODE 2
#define ATT_PRIO "s_cmp_lg_u32 s40, %[grpit]\n\t s_cbranch_scc1 L_p2_%=\n\t s_setprio 1\n\t L_p2_%=:\n\t"
#define PRIO_IT 5
#define TILE_A(VH_ADDR, KOFF, VL_ADDR, EXTRA) \
    TILE(32, 33, 34, 35, 36, 37, 38, 39, 40, 41, 42, 43, 44, 45, 46, 47, "v[32:39]", "v[56:63]", "v[48:63]", VH_ADDR, KOFF, VL_ADDR, EXTRA)
#define TILE_B(VH_ADDR, KOFF, VL_ADDR, EXTRA) \
    TILE(48, 49, 50, 51, 52, 53, 54, 55, 56, 57, 58, 59, 60, 61, 62, 63, "v[48:55]", "v[40:47]", "v[32:47]", VH_ADDR, KOFF, VL_ADDR, EXTRA)

__global__ __launch_bounds__(768, 3) void k2_main(const u16* __restrict__ ssm_k, const u16* __restrict__ ssm_D,
                                                  char* __restrict__ ws) {
    extern __shared__ __attribute__((aligned(16))) char smem[];
    const int b = blockIdx.x;
    const int tid = threadIdx.x;
    const int lane = tid & 63;
    const int w = __builtin_amdgcn_readfirstlane(tid >> 6);
    const int hh = lane >> 5;
    const int sb = b >> 3, hg = b & 7;

    STAMP_DECL STAMPR(1, 14); STAMP(1, 0);
    if (w >= 8) {
        const int cw = w - 8, ch = cw & 1;
        __builtin_amdgcn_s_setprio(3);
        u32 pf = 0;
        if (cw == 0) {
            const char* pp = ws + WS_KF + (((b >> 3) & 31) * 32 + (lane & 31)) * 128;
            asm volatile("global_load_dword %0, %1, off" : "+v"(pf) : "v"(pp) : "memory");
        }
        const u16* hrow = ssm_k + (size_t)(2 * b + ch) * SEQ;
        const u32x4 srd = {(u32)(size_t)hrow, (u32)((size_t)hrow >> 32) & 0xffffu, 4096u, 0x00020000u};
        u32x4 hn[3];
        u32 hn4[3];
#pragma unroll
        for (int p = 0; p < 3; ++p) {
            const int hi = 130 * (cw >> 1) + 64 * p + lane;
            const u32 off = (u32)(16 * (hi - 4));
            asm volatile("buffer_load_dwordx4 %0, %2, %3, 0 offen\n\tbuffer_load_dword %1, %2, %3, 0 offen offset:16"
                         : "=&v"(hn[p]), "=&v"(hn4[p]) : "v"(off), "s"(srd) : "memory");
        }
        const u16* ug = (const u16*)(ws + WS_UR) + (size_t)(2 * b + ch) * SEQ;
#pragma unroll
        for (int i = 0; i < 3; ++i) {
            const int pc = (cw >> 1) ? (i == 2 ? 4 : 3 + i) : i;
            const int z = 64 * pc + lane;
            const int P = z / 5;
            int sg = z - 5 * P;
            if (sg == 4) sg = 0;
            const int beta = ch == 0 ? 63 - P : P;
            dma16(ug + 2016 - 32 * beta + 8 * sg, smem + K2_U + (ch == 0 ? 0 : 96 * 80) + pc * 1024);
        }
        STAMP(1, 1);
        asm volatile("s_barrier" ::: "memory");
        STAMP(1, 2);
        const char* kg = ws + WS_KF + lane * 16;
        const char* vg = ws + WS_VF + lane * 16;
#pragma unroll
        for (int i = 0; i < 13; ++i) {
            const int T = 12 + i * 4 + cw;
            dma16(kg + T * 1024, smem + K2_K + T * 1024);
            dma16(vg + T * 1024, smem + K2_V + T * 1024);
        }
        STAMP(1, 6);
        asm volatile("s_waitcnt vmcnt(29)"
                     : "+v"(hn[0]), "+v"(hn[1]), "+v"(hn[2]), "+v"(hn4[0]), "+v"(hn4[1]), "+v"(hn4[2]), "+v"(pf)::"memory");
        STAMP(1, 7);
#pragma unroll
        for (int p = 0; p < 3; ++p) {
            const int hi = 130 * (cw >> 1) + 64 * p + lane;
            if (p < 2 || lane < 2) {
                u32* cE = (u32*)(smem + K2_H + ch * 8320) + 4 * hi;
                u32* cO = cE + 1040;
                u32x4 e = {hn[p][1], hn[p][2], hn[p][3], hn4[p]};
                u32x4 o = {__builtin_amdgcn_alignbit(hn[p][1], hn[p][0], 16), __builtin_amdgcn_alignbit(hn[p][2], hn[p][1], 16),
                           __builtin_amdgcn_alignbit(hn[p][3], hn[p][2], 16), __builtin_amdgcn_alignbit(hn4[p], hn[p][3], 16)};
                *(u32x4*)cE = e;
                *(u32x4*)cO = o;
            }
        }
        STAMP(1, 8);
        asm volatile("s_waitcnt vmcnt(0) lgkmcnt(0)" ::: "memory");
        STAMP(1, 9);
        {
            volatile u32* flag = (volatile u32*)(smem + K2_FLAG);
            if (lane == 0) atomicAdd((u32*)(smem + K2_FLAG), 1u);
            while (*flag < 4u) __builtin_amdgcn_s_sleep(1);
        }
        STAMP(1, 3);
        if (cw & 1) conv_wave<1>(smem, cw >> 1, lane, 2 * b + 1, ssm_D, ws);
        else conv_wave<0>(smem, cw >> 1, lane, 2 * b, ssm_D, ws);
        STAMP(1, 5); STAMPR(1, 15); STAMP_FLUSH(1);
        return;
    }
    {
        const char* kg = ws + WS_KF + lane * 16;
        const char* vg = ws + WS_VF + lane * 16;
#pragma unroll
        for (int i = 0; i < 3; ++i) {
            const int c = w * 3 + i;
            const int T = c >> 1;
            if (c & 1) dma16(vg + T * 1024, smem + K2_V + T * 1024);
            else dma16(kg + T * 1024, smem + K2_K + T * 1024);
        }
    }
    const int head = 4 * hg + (w >> 1), st = 2 * sb + (w & 1);
    const u32x4 qf = *(const u32x4*)(ws + WS_QF + ((size_t)(head * 64 + st) * 64 + lane) * 16);
    const f32x2 k2 = *(const f32x2*)(ws + WS_KM + lane * 8);
    if (tid < 160) *(u32x4*)(smem + K2_U + 64 * 80 + tid * 16) = u32x4{0, 0, 0, 0};
    if (tid == 192) *(u32*)(smem + K2_FLAG) = 0u;
    if (tid >= 256 && tid < 256 + 104) {
        const int sl = (tid - 256) & 31;
        const u32 v = (sl == 0 || sl == 4) ? 0x3f803f80u : 0u;
        *(u32x4*)(smem + K2_ONES + (tid - 256) * 16) = u32x4{v, v, v, v};
    }

    float q2 = 0.f;
#pragma unroll
    for (int i = 0; i < 4; ++i) q2 += bf_lo(qf[i]) * bf_lo(qf[i]) + bf_hi(qf[i]) * bf_hi(qf[i]);
    q2 = add_halves(q2);
    const float km = wave_max(fmaxf(k2[0], k2[1]));
    float mrow = sqrtf(q2 * km) * 1.002f + 1e-6f;
    int bar_iter = 2;
    STAMP(1, 2);
    asm volatile("s_waitcnt vmcnt(0) lgkmcnt(0)" ::: "memory");
    STAMP(1, 6);
    asm volatile("s_barrier" ::: "memory");
    STAMP(1, 3);
    if (!__all(mrow <= 48.f)) {
        {
            volatile u32* flag = (volatile u32*)(smem + K2_FLAG);
            while (*flag < 4u) __builtin_amdgcn_s_sleep(1);
        }
        bar_iter = -1;
        float mx = -3.0e38f;
        for (int T = 0; T < 64; ++T) {
            const bf16x8 kf = as_bf8(lds_rd128(smem + K2_K + T * 1024 + lane * 16));
            f32x16 s = {0};
            s = __builtin_amdgcn_mfma_f32_32x32x16_bf16(kf, as_bf8(qf), s, 0, 0, 0);
#pragma unroll
            for (int i = 0; i < 16; ++i) mx = fmaxf(mx, s[i]);
        }
        mrow = max_halves(mx);
    }
    bar_iter = __builtin_amdgcn_readfirstlane(bar_iter);
    const float negm = -mrow;
    const int row = lane & 31;
    const bool cst = row >= 16;
    const int cslot = row == 16 ? 0 : (row == 20 ? 4 : ((row > 20 && row < 28) ? 5 : 1));
    const u32 lds0 = (u32)(size_t)(lds_void_p)smem;
    u32 kaddr = lds0 + K2_K + lane * 16;
    u32 va0 = cst ? (lds0 + K2_ONES + cslot * 16) : (lds0 + K2_V + hh * 256 + row * 16);
    const u32 vinc = cst ? 0u : 4096u;
    u32 va1 = cst ? va0 : va0 + 2048;
    f32x16 O;
    const u32 flagaddr = lds0 + K2_FLAG;
    const int grp = w >> 2;
    const int grpit = grp ? PRIO_IT : -1;
    asm volatile(
        "v_mov_b32 v80, %[negm]\n\t v_mov_b32 v81, %[negm]\n\t v_mov_b32 v82, %[negm]\n\t v_mov_b32 v83, %[negm]\n\t"
        "v_mov_b32 v84, %[negm]\n\t v_mov_b32 v85, %[negm]\n\t v_mov_b32 v86, %[negm]\n\t v_mov_b32 v87, %[negm]\n\t"
        "v_mov_b32 v88, %[negm]\n\t v_mov_b32 v89, %[negm]\n\t v_mov_b32 v90, %[negm]\n\t v_mov_b32 v91, %[negm]\n\t"
        "v_mov_b32 v92, %[negm]\n\t v_mov_b32 v93, %[negm]\n\t v_mov_b32 v94, %[negm]\n\t v_mov_b32 v95, %[negm]\n\t"
        "ds_read_b128 v[100:103], v113\n\t"
        "v_mov_b32 v112, 0xdddd\n\t"
        "v_mov_b32 v64, 0\n\t v_mov_b32 v65, 0\n\t v_mov_b32 v66, 0\n\t v_mov_b32 v67, 0\n\t"
        "v_mov_b32 v68, 0\n\t v_mov_b32 v69, 0\n\t v_mov_b32 v70, 0\n\t v_mov_b32 v71, 0\n\t"
        "v_mov_b32 v72, 0\n\t v_mov_b32 v73, 0\n\t v_mov_b32 v74, 0\n\t v_mov_b32 v75, 0\n\t"
        "v_mov_b32 v76, 0\n\t v_mov_b32 v77, 0\n\t v_mov_b32 v78, 0\n\t v_mov_b32 v79, 0\n\t"
        "v_mov_b32 v56, 0\n\t v_mov_b32 v57, 0\n\t v_mov_b32 v58, 0\n\t v_mov_b32 v59, 0\n\t"
        "v_mov_b32 v60, 0\n\t v_mov_b32 v61, 0\n\t v_mov_b32 v62, 0\n\t v_mov_b32 v63, 0\n\t"
        "v_mov_b32 v108, 0\n\t v_mov_b32 v109, 0\n\t v_mov_b32 v110, 0\n\t v_mov_b32 v111, 0\n\t"
        "s_waitcnt lgkmcnt(0)\n\t"
        "v_mfma_f32_32x32x16_bf16 v[32:47], v[100:103], v[96:99], v[80:95]\n\t"
        "s_nop 7\n\t"
        "ds_read_b128 v[100:103], v113 offset:1024\n\t"
        "ds_read_b128 v[104:107], v114\n\t"
        "s_mov_b32 s40, 0\n\t"
        "s_nop 7\n\t"
        "s_waitcnt lgkmcnt(0)\n\t"
        "L_loop_%=:\n\t"
        "s_cmp_lg_u32 s40, %[bar]\n\t"
        "s_cbranch_scc1 L_nobar_%=\n\t"
        "L_spin_%=:\n\t"
        "ds_read_b32 v117, %[flag]\n\t"
        "s_waitcnt lgkmcnt(0)\n\t"
        "v_readfirstlane_b32 s41, v117\n\t"
        "s_nop 3\n\t"
        "s_cmp_ge_u32 s41, 4\n\t"
        "s_cbranch_scc1 L_nobar_%=\n\t"
        "s_sleep 1\n\t"
        "s_branch L_spin_%=\n\t"
        "L_nobar_%=:\n\t"
        ATT_PRIO
        TILE_A("v114 offset:512", "2048", "v114 offset:1024", "")
        TILE_B("v114 offset:1536", "3072", "v115", "")
        TILE_A("v115 offset:512", "4096", "v115 offset:1024", "v_add_u32 v114, v114, v116\n\t")
        TILE_B("v115 offset:1536", "5120", "v114", "")
        "v_add_u32 v113, 0x1000, v113\n\t"
        "v_add_u32 v115, v115, v116\n\t"
        "s_add_u32 s40, s40, 1\n\t"
        "s_cmp_lt_u32 s40, 16\n\t"
        "s_cbranch_scc1 L_loop_%=\n\t"
        "s_waitcnt lgkmcnt(0)\n\t"
        "v_smfmac_f32_32x32x32_bf16 v[64:79], v[108:111], v[56:63], v112\n\t"
        "s_nop 15\n\t"
        "s_nop 7\n\t"
        : "=&{v[64:79]}"(O), "+{v113}"(kaddr), "+{v114}"(va0), "+{v115}"(va1)
        : [negm] "v"(negm), [bar] "s"(bar_iter), [grp] "s"(grp), [grpit] "s"(grpit), [flag] "v"(flagaddr), "{v[96:99]}"(qf), "{v116}"(vinc)
        : "memory", "scc", "s40", "s41", "v32", "v33", "v34", "v35", "v36", "v37", "v38", "v39", "v40", "v41", "v42", "v43", "v44", "v45",
          "v46", "v47", "v48", "v49", "v50", "v51", "v52", "v53", "v54", "v55", "v56", "v57", "v58", "v59", "v60", "v61",
          "v62", "v63", "v80", "v81", "v82", "v83", "v84", "v85", "v86", "v87", "v88", "v89", "v90", "v91", "v92", "v93",
          "v94", "v95", "v100", "v101", "v102", "v103", "v104", "v105", "v106", "v107", "v108", "v109", "v110", "v111", "v112", "v117");
    STAMP(1, 4);
    {
        const float inv = 1.f / O[8];
        u32x4 o = {pk_bf16(O[0] * inv, O[1] * inv), pk_bf16(O[2] * inv, O[3] * inv), pk_bf16(O[4] * inv, O[5] * inv),
                   pk_bf16(O[6] * inv, O[7] * inv)};
        *(u32x4*)(ws + WS_OF + ((size_t)(head * 64 + st) * 64 + lane) * 16) = o;
    }
    STAMP(1, 5); STAMPR(1, 15); STAMP_FLUSH(1);
}

constexpr int K3_O = 0;
constexpr int K3_W = 65536;
constexpr int K3_EX = 131072;
constexpr int K3_LDS = 147456;

__global__ __launch_bounds__(512) void k3_out(const u16* __restrict__ x, const u16* __restrict__ wo,
                                              const char* __restrict__ ws, u16* __restrict__ out) {
    extern __shared__ __attribute__((aligned(16))) char smem[];
    const int b = blockIdx.x;
    const int sb = (b & 7) * 4 + (b >> 6);
    const int nb = (b >> 3) & 7;
    const int tid = threadIdx.x;
    const int lane = tid & 63;
    const int w = __builtin_amdgcn_readfirstlane(tid >> 6);
    const int hh = lane >> 5;
    const int nt = w & 1, st = (w >> 1) & 1, kh = w >> 2;
    const int r31 = lane & 31;

    char* wsw = const_cast<char*>(ws); (void)wsw;
    STAMP_DECL STAMPR(2, 14); STAMP(2, 0);
    const int s = 64 * sb + 32 * st + r31;
    const int n0 = 64 * nb + 32 * nt + 16 * kh + 4 * hh;
    const u32x2 xa = *(const u32x2*)(x + (size_t)s * DM + n0);
    const u32x2 xb2 = *(const u32x2*)(x + (size_t)s * DM + n0 + 8);
    const u16* yT = (const u16*)(ws + WS_YT);
    u16 yv[8];
#pragma unroll
    for (int i = 0; i < 8; ++i) yv[i] = yT[(size_t)(n0 + (i & 3) + 8 * (i >> 2)) * SEQ + s];
    const float g = ((const float*)(ws + WS_G))[s];

#pragma unroll
    for (int i = 0; i < 8; ++i) {
        const int c = w * 8 + i;
        dma16(ws + WS_OF + ((size_t)((c >> 1) * 64 + 2 * sb + (c & 1)) * 64 + lane) * 16, smem + K3_O + c * 1024);
    }
    const u16* wg = wo + nb * 64;
#pragma unroll
    for (int i = 0; i < 8; ++i) {
        const int k0 = (w * 8 + i) * 8;
        const int k = k0 + (lane >> 3);
        const int cp = lane & 7;
        dma16(wg + k * DM + 8 * (cp ^ (4 * ((k >> 1) & 1))), smem + K3_W + k0 * 128);
    }
    STAMP(2, 1);
    asm volatile("s_waitcnt vmcnt(0)" ::: "memory");
    STAMP(2, 2);
    __syncthreads();
    STAMP(2, 3);

    const int blk = (lane >> 4) & 1, q = (lane & 15) >> 2, p = lane & 3;
    const char* ob = smem + K3_O + (kh * 32 + st) * 1024 + lane * 16;
    const char* wb = smem + K3_W + (kh * 256 + 4 * hh + q) * 128 + 64 * (nt ^ ((q >> 1) & 1)) + 32 * blk + 8 * p;
    f32x16 acc = {0};
#pragma unroll
    for (int i = 0; i < 16; ++i) {
        const bf16x8 of = as_bf8(lds_rd128(ob + i * 2048));
        const bf16x8 wf = cat_tr(lds_tr(wb + i * 2048), lds_tr(wb + i * 2048 + 1024));
        acc = __builtin_amdgcn_mfma_f32_32x32x16_bf16(wf, of, acc, 0, 0, 0);
    }
    char* ex = smem + K3_EX;
    f32x4 s0, s1, k0, k1;
    if (kh == 0) {
        s0 = f32x4{acc[8], acc[9], acc[10], acc[11]};
        s1 = f32x4{acc[12], acc[13], acc[14], acc[15]};
        k0 = f32x4{acc[0], acc[1], acc[2], acc[3]};
        k1 = f32x4{acc[4], acc[5], acc[6], acc[7]};
    } else {
        s0 = f32x4{acc[0], acc[1], acc[2], acc[3]};
        s1 = f32x4{acc[4], acc[5], acc[6], acc[7]};
        k0 = f32x4{acc[8], acc[9], acc[10], acc[11]};
        k1 = f32x4{acc[12], acc[13], acc[14], acc[15]};
    }
    *(f32x4*)(ex + w * 2048 + lane * 16) = s0;
    *(f32x4*)(ex + w * 2048 + 1024 + lane * 16) = s1;
    __syncthreads();
    k0 += *(const f32x4*)(ex + (w ^ 4) * 2048 + lane * 16);
    k1 += *(const f32x4*)(ex + (w ^ 4) * 2048 + 1024 + lane * 16);
    const float g1 = 1.f - g;
    float o[8];
    o[0] = g * bf_lo(xa[0]) * bf_lo((u32)yv[0]) + g1 * k0[0];
    o[1] = g * bf_hi(xa[0]) * bf_lo((u32)yv[1]) + g1 * k0[1];
    o[2] = g * bf_lo(xa[1]) * bf_lo((u32)yv[2]) + g1 * k0[2];
    o[3] = g * bf_hi(xa[1]) * bf_lo((u32)yv[3]) + g1 * k0[3];
    o[4] = g * bf_lo(xb2[0]) * bf_lo((u32)yv[4]) + g1 * k1[0];
    o[5] = g * bf_hi(xb2[0]) * bf_lo((u32)yv[5]) + g1 * k1[1];
    o[6] = g * bf_lo(xb2[1]) * bf_lo((u32)yv[6]) + g1 * k1[2];
    o[7] = g * bf_hi(xb2[1]) * bf_lo((u32)yv[7]) + g1 * k1[3];
    u32 a0 = pk_bf16(o[0], o[1]), a1 = pk_bf16(o[2], o[3]), b0 = pk_bf16(o[4], o[5]), b1 = pk_bf16(o[6], o[7]);
    auto r0 = __builtin_amdgcn_permlane32_swap(a0, b0, false, false);
    auto r1 = __builtin_amdgcn_permlane32_swap(a1, b1, false, false);
    u32x4 ov = {r0[0], r1[0], r0[1], r1[1]};
    *(u32x4*)(out + (size_t)s * DM + 64 * nb + 32 * nt + 16 * kh + 8 * hh) = ov;
    STAMP(2, 5); STAMPR(2, 15); STAMP_FLUSH(2);
}

extern "C" void kernel_launch(void* const* d_in, const int* in_sizes, int n_in, void* d_out, int out_size, void* d_ws,
                              size_t ws_size, hipStream_t stream) {
    const u16* A_q = (const u16*)d_in[0];
    const u16* A_k = (const u16*)d_in[1];
    const u16* A_v = (const u16*)d_in[2];
    const u16* shift_k = (const u16*)d_in[3];
    const u16* ssm_k = (const u16*)d_in[4];
    const u16* ssm_D = (const u16*)d_in[5];
    const u16* wq = (const u16*)d_in[7];
    const u16* wk = (const u16*)d_in[8];
    const u16* wv = (const u16*)d_in[9];
    const u16* wo = (const u16*)d_in[10];
    const u16* gate_w = (const u16*)d_in[11];
    char* ws = (char*)d_ws;
    (void)hipFuncSetAttribute((const void*)k1_prep, hipFuncAttributeMaxDynamicSharedMemorySize, K1_LDS);
    (void)hipFuncSetAttribute((const void*)k2_main, hipFuncAttributeMaxDynamicSharedMemorySize, K2_LDS);
    (void)hipFuncSetAttribute((const void*)k3_out, hipFuncAttributeMaxDynamicSharedMemorySize, K3_LDS);
    k1_prep<<<256, 512, K1_LDS, stream>>>(A_q, A_k, A_v, shift_k, wq, wk, wv, gate_w, ws);
    k2_main<<<256, 768, K2_LDS, stream>>>(ssm_k, ssm_D, ws);
    k3_out<<<256, 512, K3_LDS, stream>>>(A_q, wo, ws, (u16*)d_out);
}
```

```cpp
#include <hip/hip_runtime.h>
#include <hip/hip_bf16.h>
#include <stdint.h>

typedef unsigned short u16;
typedef unsigned int u32;
typedef unsigned long long u64;
typedef __attribute__((ext_vector_type(8))) __bf16 bf16x8;
typedef __attribute__((ext_vector_type(4))) short s16x4;
typedef __attribute__((ext_vector_type(16))) float f32x16;
typedef __attribute__((ext_vector_type(4))) float f32x4;
typedef __attribute__((ext_vector_type(4))) u32 u32x4;
typedef __attribute__((ext_vector_type(2))) u32 u32x2;
typedef __attribute__((ext_vector_type(2))) float f32x2;
typedef __attribute__((ext_vector_type(2))) __bf16 bf16x2;
typedef __attribute__((address_space(3))) s16x4* lds_s16x4_p;
typedef __attribute__((address_space(3))) void* lds_void_p;
typedef __attribute__((address_space(3))) u32 lds_u32;
typedef const __attribute__((address_space(1))) void* glb_cvoid_p;
typedef __attribute__((address_space(1))) u32 gu32;
typedef __attribute__((address_space(1))) u64 gu64;

#define DEV __device__ __forceinline__
#define RLX_AGENT __ATOMIC_RELAXED, __HIP_MEMORY_SCOPE_AGENT
#define STR2(x) #x
#define STR(x) STR2(x)

#define STAMP_DECL
#define STAMP(idx)
#define STAMPR(idx)
#define STAMP_FLUSH()

constexpr int SEQ = 2048;
constexpr int DM = 512;

constexpr size_t WS_CNT = 0;
constexpr int CNT_LINES = 160;
constexpr size_t WS_KF = 32768;
constexpr size_t WS_VF = WS_KF + 65536;
constexpr size_t WS_KM = WS_VF + 65536;
constexpr size_t WS_OF = 262144;
constexpr size_t WS_UR = WS_OF + 2097152;
constexpr size_t WS_YT = WS_UR + 2097152;
constexpr int C_KV = 0;
constexpr int C_T = 64;
constexpr int C_O = 96;
constexpr int C_Y = 128;

constexpr int L_X = 0;
constexpr int L_W = 66560;
constexpr int L_WKV = 132096;
constexpr int L_GW = 149504;
constexpr int L_EX1 = 132736;
constexpr int L_TS = 16384;
constexpr int L_K = 0;
constexpr int L_V = 65536;
constexpr int L_ONES = 131072;
constexpr int L_H = L_ONES + 1664;
constexpr int L_U = L_H + 4 * 4160;
constexpr int L_O = 0;
constexpr int L_WO = 65536;
constexpr int L_EX5 = 131072;
constexpr int L_FLAG = L_U + 160 * 80;
constexpr int L_G = L_FLAG + 32;
constexpr int LDS_BYTES = L_G + 256;
static_assert(LDS_BYTES <= 163840, "lds");
static_assert((L_U & 15) == 0 && (L_H & 15) == 0 && (L_FLAG & 15) == 0, "align");

DEV float bf_lo(u32 v) { return __builtin_bit_cast(float, v << 16); }
DEV float bf_hi(u32 v) { return __builtin_bit_cast(float, v & 0xffff0000u); }
DEV u32 pk_bf16(float a, float b) {
    f32x2 f = {a, b};
    bf16x2 r = __builtin_convertvector(f, bf16x2);
    return __builtin_bit_cast(u32, r);
}
DEV void dma16(const void* g, char* lds) {
    __builtin_amdgcn_global_load_lds((glb_cvoid_p)g, (lds_void_p)lds, 16, 0, 0);
}
DEV void dma16_dev(const void* g, char* lds) {
    __builtin_amdgcn_global_load_lds((glb_cvoid_p)g, (lds_void_p)lds, 16, 0, 16);
}
DEV bf16x8 as_bf8(u32x4 v) { return __builtin_bit_cast(bf16x8, v); }
DEV float dot2(u32 a, u32 b, float c) {
    return __builtin_amdgcn_fdot2_f32_bf16(__builtin_bit_cast(bf16x2, a), __builtin_bit_cast(bf16x2, b), c, false);
}
DEV u32x4 lds_rd128(const char* p) { return *(const u32x4*)p; }
DEV u32x2 lds_tr(const char* p) {
    s16x4 v = __builtin_amdgcn_ds_read_tr16_b64_v4i16((lds_s16x4_p)(p));
    return __builtin_bit_cast(u32x2, v);
}
DEV bf16x8 cat_tr(u32x2 a, u32x2 b) {
    u32x4 r = {a[0], a[1], b[0], b[1]};
    return as_bf8(r);
}
template <int CTRL>
DEV float dpp_mov(float old, float v) {
    return __builtin_bit_cast(float, __builtin_amdgcn_update_dpp(__builtin_bit_cast(int, old), __builtin_bit_cast(int, v), CTRL, 0xf, 0xf, false));
}
DEV float wave_max(float v) {
    v = fmaxf(v, dpp_mov<0x111>(v, v));
    v = fmaxf(v, dpp_mov<0x112>(v, v));
    v = fmaxf(v, dpp_mov<0x114>(v, v));
    v = fmaxf(v, dpp_mov<0x118>(v, v));
    v = fmaxf(v, dpp_mov<0x142>(v, v));
    v = fmaxf(v, dpp_mov<0x143>(v, v));
    return __builtin_bit_cast(float, __builtin_amdgcn_readlane(__builtin_bit_cast(int, v), 63));
}
DEV float add_halves(float v) {
    const u32 x = __builtin_bit_cast(u32, v);
    auto r = __builtin_amdgcn_permlane32_swap(x, x, false, false);
    return __builtin_bit_cast(float, (u32)r[0]) + __builtin_bit_cast(float, (u32)r[1]);
}
DEV float add_rows16(float v) {
    const u32 x = __builtin_bit_cast(u32, v);
    auto r = __builtin_amdgcn_permlane16_swap(x, x, false, false);
    return __builtin_bit_cast(float, (u32)r[0]) + __builtin_bit_cast(float, (u32)r[1]);
}
DEV float max_halves(float v) {
    const u32 x = __builtin_bit_cast(u32, v);
    auto r = __builtin_amdgcn_permlane32_swap(x, x, false, false);
    return fmaxf(__builtin_bit_cast(float, (u32)r[0]), __builtin_bit_cast(float, (u32)r[1]));
}
#define BAR() asm volatile("s_waitcnt lgkmcnt(0)\n\ts_barrier" ::: "memory")
#define DRAIN() asm volatile("s_waitcnt vmcnt(0)" ::: "memory")

constexpr u32 SPIN_CAP = 1u << 17;
DEV gu32* cnt_ptr(char* ws, int line) { return (gu32*)(ws + WS_CNT + (size_t)line * 128); }
DEV void poll_counters(char* ws, int line, u32 want) {
    gu32* p = cnt_ptr(ws, line);
#pragma nounroll
    for (u32 spins = 0; spins < SPIN_CAP; ++spins) {
        const u32 v = __hip_atomic_load(p, RLX_AGENT);
        if (__all(v >= want)) break;
        __builtin_amdgcn_s_sleep(1);
    }
    __builtin_amdgcn_fence(__ATOMIC_ACQUIRE, "wavefront");
    asm volatile("" ::: "memory");
}
DEV void lds_wait_ge(char* smem, int off, u32 want) {
    volatile lds_u32* f = (volatile lds_u32*)(lds_void_p)(smem + off);
#pragma nounroll
    for (u32 spins = 0; spins < (SPIN_CAP << 4); ++spins) {
        if (*f >= want) break;
        __builtin_amdgcn_s_sleep(1);
    }
    asm volatile("" ::: "memory");
}
DEV void lds_add(char* smem, int off) {
    __hip_atomic_fetch_add((lds_u32*)(lds_void_p)(smem + off), 1u, __ATOMIC_RELAXED, __HIP_MEMORY_SCOPE_WORKGROUP);
}

#define GM_RD(A0, A1, B, I, TOFF, BSTEP)                                   \
    "ds_read_b64_tr_b16 " A0 ", v72 offset:" #I "*2048\n\t"                \
    "ds_read_b64_tr_b16 " A1 ", v72 offset:" #I "*2048+" TOFF "\n\t"       \
    "ds_read_b128 " B ", v73 offset:" #I "*" BSTEP "\n\t"
#define GM_R0(I, T, S) GM_RD("v[32:33]", "v[34:35]", "v[36:39]", I, T, S)
#define GM_R1(I, T, S) GM_RD("v[40:41]", "v[42:43]", "v[44:47]", I, T, S)
#define GM_R2(I, T, S) GM_RD("v[48:49]", "v[50:51]", "v[52:55]", I, T, S)
#define GM_R3(I, T, S) GM_RD("v[56:57]", "v[58:59]", "v[60:63]", I, T, S)
#define GM_R4(I, T, S) GM_RD("v[64:65]", "v[66:67]", "v[68:71]", I, T, S)
#define GM_M(W, A, B, C) "s_waitcnt lgkmcnt(" #W ")\n\tv_mfma_f32_32x32x16_bf16 v[16:31], " A ", " B ", " C "\n\t"
#define GM_M0(W, C) GM_M(W, "v[32:35]", "v[36:39]", C)
#define GM_M1(W, C) GM_M(W, "v[40:43]", "v[44:47]", C)
#define GM_M2(W, C) GM_M(W, "v[48:51]", "v[52:55]", C)
#define GM_M3(W, C) GM_M(W, "v[56:59]", "v[60:63]", C)
#define GM_M4(W, C) GM_M(W, "v[64:67]", "v[68:71]", C)
#define GM_ACC "v[16:31]"
#define GEMM16(T, S)                                                                                   \
    GM_R0(0, T, S) GM_R1(1, T, S) GM_R2(2, T, S) GM_R3(3, T, S)                                        \
    GM_R4(4, T, S) GM_M0(12, "0") GM_R0(5, T, S) GM_M1(12, GM_ACC) GM_R1(6, T, S) GM_M2(12, GM_ACC)    \
    GM_R2(7, T, S) GM_M3(12, GM_ACC) GM_R3(8, T, S) GM_M4(12, GM_ACC) GM_R4(9, T, S) GM_M0(12, GM_ACC) \
    GM_R0(10, T, S) GM_M1(12, GM_ACC) GM_R1(11, T, S) GM_M2(12, GM_ACC) GM_R2(12, T, S) GM_M3(12, GM_ACC) \
    GM_R3(13, T, S) GM_M4(12, GM_ACC) GM_R4(14, T, S) GM_M0(12, GM_ACC) GM_R0(15, T, S) GM_M1(12, GM_ACC) \
    GM_M2(9, GM_ACC) GM_M3(6, GM_ACC) GM_M4(3, GM_ACC) GM_M0(0, GM_ACC)                                \
    "s_nop 15\n\t"
#define GEMM_CLOB                                                                                        \
    "memory", "v32", "v33", "v34", "v35", "v36", "v37", "v38", "v39", "v40", "v41", "v42", "v43", "v44", \
    "v45", "v46", "v47", "v48", "v49", "v50", "v51", "v52", "v53", "v54", "v55", "v56", "v57", "v58",   \
    "v59", "v60", "v61", "v62", "v63", "v64", "v65", "v66", "v67", "v68", "v69", "v70", "v71"
DEV f32x16 gemm_proj(u32 aaddr, u32 baddr) {
    f32x16 acc;
    asm volatile(GEMM16("512", "32") : "=&{v[16:31]}"(acc) : "{v72}"(aaddr), "{v73}"(baddr) : GEMM_CLOB);
    return acc;
}
DEV f32x16 gemm_out(u32 aaddr, u32 baddr) {
    f32x16 acc;
    asm volatile(GEMM16("1024", "2048") : "=&{v[16:31]}"(acc) : "{v72}"(aaddr), "{v73}"(baddr) : GEMM_CLOB);
    return acc;
}
DEV void exch_write(char* ex, int w, int lane, int kh, const f32x16& acc, f32x4& k0, f32x4& k1) {
    f32x4 s0, s1;
    if (kh == 0) {
        s0 = f32x4{acc[8], acc[9], acc[10], acc[11]};
        s1 = f32x4{acc[12], acc[13], acc[14], acc[15]};
        k0 = f32x4{acc[0], acc[1], acc[2], acc[3]};
        k1 = f32x4{acc[4], acc[5], acc[6], acc[7]};
    } else {
        s0 = f32x4{acc[0], acc[1], acc[2], acc[3]};
        s1 = f32x4{acc[4], acc[5], acc[6], acc[7]};
        k0 = f32x4{acc[8], acc[9], acc[10], acc[11]};
        k1 = f32x4{acc[12], acc[13], acc[14], acc[15]};
    }
    *(f32x4*)(ex + w * 2048 + lane * 16) = s0;
    *(f32x4*)(ex + w * 2048 + 1024 + lane * 16) = s1;
}

#define CV_RD(SET_A0, SET_A1, SET_B, MI, UOFF)                                                    \
    "ds_read2_b32 " SET_A0 ", v64 offset0:" #MI "*8 offset1:" #MI "*8+1\n\t"                      \
    "ds_read2_b32 " SET_A1 ", v64 offset0:" #MI "*8+2 offset1:" #MI "*8+3\n\t"                    \
    "ds_read_b128 " SET_B ", v65 offset:" UOFF "\n\t"
#define CV_RD0(MI, UOFF) CV_RD("v[32:33]", "v[34:35]", "v[36:39]", MI, UOFF)
#define CV_RD1(MI, UOFF) CV_RD("v[40:41]", "v[42:43]", "v[44:47]", MI, UOFF)
#define CV_RD2(MI, UOFF) CV_RD("v[48:49]", "v[50:51]", "v[52:55]", MI, UOFF)
#define CV_RD3(MI, UOFF) CV_RD("v[56:57]", "v[58:59]", "v[60:63]", MI, UOFF)
#define CV_MM(A, B) "s_waitcnt lgkmcnt(9)\n\tv_mfma_f32_32x32x16_bf16 v[16:31], " A ", " B ", v[16:31]\n\t" CV_PACE
#define CV_MM0 CV_MM("v[32:35]", "v[36:39]")
#define CV_MM1 CV_MM("v[40:43]", "v[44:47]")
#define CV_MM2 CV_MM("v[48:51]", "v[52:55]")
#define CV_MM3 CV_MM("v[56:59]", "v[60:63]")
#define CONV_SLEEP 0
#define CONV_PRIO 0
#define CV_PACE ""
#define CV_BODY(UO)                                                        \
    CV_RD3(2, UO(1) "+0")  CV_MM0                                           \
    CV_RD0(5, UO(2) "+32") CV_MM1                                           \
    CV_RD1(4, UO(2) "+0")  CV_MM2                                           \
    CV_RD2(7, UO(3) "+32") CV_MM3                                           \
    CV_RD3(6, UO(3) "+0")  CV_MM0                                           \
    CV_RD0(9, UO(4) "+32") CV_MM1                                           \
    CV_RD1(8, UO(4) "+0")  CV_MM2                                           \
    CV_RD2(11, UO(5) "+32") CV_MM3                                          \
    CV_RD3(10, UO(5) "+0") CV_MM0                                           \
    CV_RD0(13, UO(6) "+32") CV_MM1                                          \
    CV_RD1(12, UO(6) "+0") CV_MM2                                           \
    CV_RD2(15, UO(7) "+32") CV_MM3                                          \
    CV_RD3(14, UO(7) "+0") CV_MM0                                           \
    CV_RD0(17, UO(8) "+32") CV_MM1                                          \
    CV_RD1(16, UO(8) "+0") CV_MM2                                           \
    CV_RD2(19, UO(9) "+32") CV_MM3
#define CV_UO0(dd) "80*" #dd
#define CV_UO1(dd) "80*(9-" #dd ")"
#define CV_ASM(UO, USTEP)                                                                          \
    asm volatile(                                                                                  \
        "s_setprio " STR(CONV_PRIO) "\n\t"                                                         \
        "v_mov_b32 v16, 0\n\t v_mov_b32 v17, 0\n\t v_mov_b32 v18, 0\n\t v_mov_b32 v19, 0\n\t"     \
        "v_mov_b32 v20, 0\n\t v_mov_b32 v21, 0\n\t v_mov_b32 v22, 0\n\t v_mov_b32 v23, 0\n\t"     \
        "v_mov_b32 v24, 0\n\t v_mov_b32 v25, 0\n\t v_mov_b32 v26, 0\n\t v_mov_b32 v27, 0\n\t"     \
        "v_mov_b32 v28, 0\n\t v_mov_b32 v29, 0\n\t v_mov_b32 v30, 0\n\t v_mov_b32 v31, 0\n\t"     \
        CV_RD0(1, UO(0) "+32") CV_RD1(0, UO(0) "+0") CV_RD2(3, UO(1) "+32")                        \
        "s_mov_b32 s41, %[n]\n\t"                                                                  \
        "L_cv_%=:\n\t"                                                                             \
        CV_BODY(UO)                                                                                \
        "v_add_u32 v64, 0x200, v64\n\t"                                                            \
        "v_add_u32 v65, " USTEP ", v65\n\t"                                                        \
        "s_sub_u32 s41, s41, 1\n\t"                                                                \
        "s_cmp_lg_u32 s41, 0\n\t"                                                                  \
        "s_cbranch_scc1 L_cv_%=\n\t"                                                               \
        "s_waitcnt lgkmcnt(0)\n\t"                                                                 \
        "s_setprio 1\n\t"                                                                          \
        "s_nop 15\n\t"                                                                             \
        : "=&{v[16:31]}"(acc), "+{v64}"(ha), "+{v65}"(ua)                                          \
        : [n] "s"(nit)                                                                             \
        : "memory", "scc", "s41", "v32", "v33", "v34", "v35", "v36", "v37", "v38", "v39", "v40", "v41", "v42", "v43", \
          "v44", "v45", "v46", "v47", "v48", "v49", "v50", "v51", "v52", "v53", "v54", "v55", "v56", "v57", "v58", "v59", \
          "v60", "v61", "v62", "v63")

template <int CH>
DEV void conv_wave(char* smem, int nt, int lane, int ch_global, float Dv, char* __restrict__ ws) {
    const int r = lane & 31, hh = lane >> 5;
    const u32 lds0 = (u32)(size_t)(lds_void_p)smem;
    u32 ha = lds0 + L_H + CH * 8320 + ((r & 1) ? 0 : 4160) + 16 * hh + 4 * (r >> 1);
    u32 ua;
    if (CH == 0) ua = lds0 + L_U + (63 - 32 * nt - r) * 80 + 16 * hh;
    else ua = lds0 + L_U + 96 * 80 + (32 * nt + r - 9) * 80 + 16 * hh;
    const int nit = __builtin_amdgcn_readfirstlane(4 * (nt + 1));
    f32x16 acc;
    if (CH == 0) CV_ASM(CV_UO0, "0x280");
    else CV_ASM(CV_UO1, "0xfffffd80");
    const int beta = 32 * nt + r;
    const char* ublk = (CH == 0) ? (smem + L_U + (63 - beta) * 80) : (smem + L_U + 96 * 80 + beta * 80);
    u32 pk[8];
#pragma unroll
    for (int qd = 0; qd < 4; ++qd) {
        const u32x2 uu = *(const u32x2*)(ublk + 2 * (28 - 8 * qd - 4 * hh));
        pk[2 * qd] = pk_bf16(acc[4 * qd + 0] + Dv * bf_hi(uu[1]), acc[4 * qd + 1] + Dv * bf_lo(uu[1]));
        pk[2 * qd + 1] = pk_bf16(acc[4 * qd + 2] + Dv * bf_hi(uu[0]), acc[4 * qd + 3] + Dv * bf_lo(uu[0]));
    }
    const __amdgpu_buffer_rsrc_t yr = __builtin_amdgcn_make_buffer_rsrc(ws + WS_YT, 0, 2097152, 0x00020000);
#pragma unroll
    for (int pr = 0; pr < 2; ++pr) {
        auto r0 = __builtin_amdgcn_permlane32_swap(pk[4 * pr], pk[4 * pr + 2], false, false);
        auto r1 = __builtin_amdgcn_permlane32_swap(pk[4 * pr + 1], pk[4 * pr + 3], false, false);
        u32x4 ov = {r0[0], r1[0], r0[1], r1[1]};
        __builtin_amdgcn_raw_buffer_store_b128(ov, yr, (u32)((ch_global * SEQ + 32 * beta + 16 * pr + 8 * hh) * 2), 0, 16);
    }
}

#define E_(r) "v_exp_f32 v" #r ", v" #r "\n\t"
#define TILE(C0, C1, C2, C3, C4, C5, C6, C7, C8, C9, C10, C11, C12, C13, C14, C15, CLO, NHI, NALL, VH_ADDR, KOFF, VL_ADDR, EXTRA) \
    E_(C0) E_(C1)                                                                                   \
    "s_waitcnt lgkmcnt(2)\n\t"                                                                      \
    "v_smfmac_f32_32x32x32_bf16 v[64:79], v[108:111], " NHI ", v112\n\t"                            \
    "ds_read_b128 v[108:111], " VH_ADDR "\n\t"                                                      \
    E_(C2) E_(C3) E_(C4)                                                                            \
    "s_waitcnt lgkmcnt(2)\n\t"                                                                      \
    "v_mfma_f32_32x32x16_bf16 " NALL ", v[100:103], v[96:99], v[80:95]\n\t"                         \
    "ds_read_b128 v[100:103], v113 offset:" KOFF "\n\t"                                             \
    E_(C5) E_(C6) E_(C7) E_(C8) EXTRA E_(C9) E_(C10)                                                \
    "s_waitcnt lgkmcnt(2)\n\t"                                                                      \
    "v_smfmac_f32_32x32x32_bf16 v[64:79], v[104:107], " CLO ", v112\n\t"                            \
    "ds_read_b128 v[104:107], " VL_ADDR "\n\t"                                                      \
    E_(C11) E_(C12) E_(C13) E_(C14) E_(C15)
#define TILE_A(VH_ADDR, KOFF, VL_ADDR, EXTRA) \
    TILE(32, 33, 34, 35, 36, 37, 38, 39, 40, 41, 42, 43, 44, 45, 46, 47, "v[32:39]", "v[56:63]", "v[48:63]", VH_ADDR, KOFF, VL_ADDR, EXTRA)
#define TILE_B(VH_ADDR, KOFF, VL_ADDR, EXTRA) \
    TILE(48, 49, 50, 51, 52, 53, 54, 55, 56, 57, 58, 59, 60, 61, 62, 63, "v[48:55]", "v[40:47]", "v[32:47]", VH_ADDR, KOFF, VL_ADDR, EXTRA)
#define PRIO_IT 5

DEV f32x16 attn_loop(u32x4 qf, float negm, u32 kaddr, u32 va0, u32 va1, u32 vinc, u32 flagaddr, int wait_iter, int prio_iter) {
    f32x16 O;
    asm volatile(
        "v_mov_b32 v80, %[negm]\n\t v_mov_b32 v81, %[negm]\n\t v_mov_b32 v82, %[negm]\n\t v_mov_b32 v83, %[negm]\n\t"
        "v_mov_b32 v84, %[negm]\n\t v_mov_b32 v85, %[negm]\n\t v_mov_b32 v86, %[negm]\n\t v_mov_b32 v87, %[negm]\n\t"
        "v_mov_b32 v88, %[negm]\n\t v_mov_b32 v89, %[negm]\n\t v_mov_b32 v90, %[negm]\n\t v_mov_b32 v91, %[negm]\n\t"
        "v_mov_b32 v92, %[negm]\n\t v_mov_b32 v93, %[negm]\n\t v_mov_b32 v94, %[negm]\n\t v_mov_b32 v95, %[negm]\n\t"
        "ds_read_b128 v[100:103], v113\n\t"
        "v_mov_b32 v112, 0xdddd\n\t"
        "v_mov_b32 v64, 0\n\t v_mov_b32 v65, 0\n\t v_mov_b32 v66, 0\n\t v_mov_b32 v67, 0\n\t"
        "v_mov_b32 v68, 0\n\t v_mov_b32 v69, 0\n\t v_mov_b32 v70, 0\n\t v_mov_b32 v71, 0\n\t"
        "v_mov_b32 v72, 0\n\t v_mov_b32 v73, 0\n\t v_mov_b32 v74, 0\n\t v_mov_b32 v75, 0\n\t"
        "v_mov_b32 v76, 0\n\t v_mov_b32 v77, 0\n\t v_mov_b32 v78, 0\n\t v_mov_b32 v79, 0\n\t"
        "v_mov_b32 v56, 0\n\t v_mov_b32 v57, 0\n\t v_mov_b32 v58, 0\n\t v_mov_b32 v59, 0\n\t"
        "v_mov_b32 v60, 0\n\t v_mov_b32 v61, 0\n\t v_mov_b32 v62, 0\n\t v_mov_b32 v63, 0\n\t"
        "v_mov_b32 v108, 0\n\t v_mov_b32 v109, 0\n\t v_mov_b32 v110, 0\n\t v_mov_b32 v111, 0\n\t"
        "s_waitcnt lgkmcnt(0)\n\t"
        "v_mfma_f32_32x32x16_bf16 v[32:47], v[100:103], v[96:99], v[80:95]\n\t"
        "s_nop 7\n\t"
        "ds_read_b128 v[100:103], v113 offset:1024\n\t"
        "ds_read_b128 v[104:107], v114\n\t"
        "s_mov_b32 s40, 0\n\t"
        "s_nop 7\n\t"
        "s_waitcnt lgkmcnt(0)\n\t"
        "L_loop_%=:\n\t"
        "s_cmp_lg_u32 s40, %[wit]\n\t"
        "s_cbranch_scc1 L_nowait_%=\n\t"
        "s_mov_b32 s42, 0x200000\n\t"
        "L_spin_%=:\n\t"
        "ds_read_b32 v117, %[flag]\n\t"
        "s_waitcnt lgkmcnt(0)\n\t"
        "v_readfirstlane_b32 s41, v117\n\t"
        "s_sub_u32 s42, s42, 1\n\t"
        "s_cbranch_scc1 L_nowait_%=\n\t"
        "s_nop 1\n\t"
        "s_cmp_ge_u32 s41, 4\n\t"
        "s_cbranch_scc1 L_nowait_%=\n\t"
        "s_sleep 1\n\t"
        "s_branch L_spin_%=\n\t"
        "L_nowait_%=:\n\t"
        "s_cmp_lg_u32 s40, %[pit]\n\t"
        "s_cbranch_scc1 L_noprio_%=\n\t"
        "s_setprio 1\n\t"
        "L_noprio_%=:\n\t"
        TILE_A("v114 offset:512", "2048", "v114 offset:1024", "")
        TILE_B("v114 offset:1536", "3072", "v115", "")
        TILE_A("v115 offset:512", "4096", "v115 offset:1024", "v_add_u32 v114, v114, v116\n\t")
        TILE_B("v115 offset:1536", "5120", "v114", "")
        "v_add_u32 v113, 0x1000, v113\n\t"
        "v_add_u32 v115, v115, v116\n\t"
        "s_add_u32 s40, s40, 1\n\t"
        "s_cmp_lt_u32 s40, 16\n\t"
        "s_cbranch_scc1 L_loop_%=\n\t"
        "s_waitcnt lgkmcnt(0)\n\t"
        "v_smfmac_f32_32x32x32_bf16 v[64:79], v[108:111], v[56:63], v112\n\t"
        "s_setprio 0\n\t"
        "s_nop 15\n\t"
        "s_nop 7\n\t"
        : "=&{v[64:79]}"(O), "+{v113}"(kaddr), "+{v114}"(va0), "+{v115}"(va1)
        : [negm] "v"(negm), [wit] "s"(wait_iter), [pit] "s"(prio_iter), [flag] "v"(flagaddr), "{v[96:99]}"(qf), "{v116}"(vinc)
        : "memory", "scc", "s40", "s41", "s42", "v32", "v33", "v34", "v35", "v36", "v37", "v38", "v39", "v40", "v41", "v42", "v43", "v44",
          "v45", "v46", "v47", "v48", "v49", "v50", "v51", "v52", "v53", "v54", "v55", "v56", "v57", "v58", "v59", "v60", "v61",
          "v62", "v63", "v80", "v81", "v82", "v83", "v84", "v85", "v86", "v87", "v88", "v89", "v90", "v91", "v92", "v93",
          "v94", "v95", "v100", "v101", "v102", "v103", "v104", "v105", "v106", "v107", "v108", "v109", "v110", "v111", "v112", "v117");
    return O;
}

__global__ void k_zero(char* __restrict__ ws) {
    if (threadIdx.x < CNT_LINES) *(u32*)(ws + WS_CNT + (size_t)threadIdx.x * 128) = 0u;
}

__global__ __launch_bounds__(768, 3) void k_mega(const u16* __restrict__ x, const u16* __restrict__ kin,
                                                 const u16* __restrict__ vin, const u16* __restrict__ shift_k,
                                                 const u16* __restrict__ ssm_k, const u16* __restrict__ ssm_D,
                                                 const u16* __restrict__ wq, const u16* __restrict__ wk,
                                                 const u16* __restrict__ wv, const u16* __restrict__ wo,
                                                 const u16* __restrict__ gate_w, char* __restrict__ ws,
                                                 u16* __restrict__ out) {
    extern __shared__ __attribute__((aligned(16))) char smem[];
    const int b = blockIdx.x;
    const int sb = (b & 7) * 4 + (b >> 6);
    const int hg = (b >> 3) & 7;
    const int tid = threadIdx.x;
    const int lane = tid & 63;
    const int w = __builtin_amdgcn_readfirstlane(tid >> 6);
    const int hh = lane >> 5;
    const u32 lds0 = (u32)(size_t)(lds_void_p)smem;
    STAMP_DECL STAMPR(22); STAMP(0);

    if (w >= 8) {
        const int cw = w - 8, ch = cw & 1;
        const int chg = 2 * b + ch;
        __builtin_amdgcn_s_setprio(3);
        const int kg = hg >> 1;
        if (tid == 576) { *(u32x4*)(smem + L_FLAG) = u32x4{0, 0, 0, 0}; *(u32x4*)(smem + L_FLAG + 16) = u32x4{0, 0, 0, 0}; }
        if (cw == 0) {
            const u16* wkv = (hg & 1) ? wv : wk;
#pragma unroll
            for (int i = 0; i < 16; ++i) dma16(wkv + i * 512 + lane * 8, smem + L_WKV + i * 1024);
            const u16* xg = x + (size_t)(64 * sb + 16 * kg) * DM + lane * 8;
#pragma unroll
            for (int i = 0; i < 16; ++i) dma16(xg + i * DM, smem + L_X + (16 * kg + i) * 1040);
            DRAIN();
        }
        STAMP(1);
        if (cw == 0) {
            const int g = lane >> 4, c16 = lane & 15;
            const int q = c16 >> 2, p = lane & 3;
            const char* xb = smem + L_X + (16 * kg + c16) * 1040 + 16 * g;
            const char* wb = smem + L_WKV + (8 * g + q) * 32 + 8 * p;
            bf16x8 xf[16], wf[16];
#pragma unroll
            for (int i = 0; i < 16; ++i) {
                xf[i] = as_bf8(lds_rd128(xb + i * 64));
                wf[i] = cat_tr(lds_tr(wb + i * 1024), lds_tr(wb + i * 1024 + 128));
            }
            __builtin_amdgcn_sched_barrier(0);
            f32x4 acc = {0.f, 0.f, 0.f, 0.f};
            const int T = 2 * sb + (kg >> 1);
            if (hg & 1) {
#pragma unroll
                for (int i = 0; i < 16; ++i) acc = __builtin_amdgcn_mfma_f32_16x16x32_bf16(xf[i], wf[i], acc, 0, 0, 0);
                const u64 o = (u64)pk_bf16(acc[0], acc[1]) | ((u64)pk_bf16(acc[2], acc[3]) << 32);
                char* dst = ws + WS_VF + (size_t)(T * 2 + (kg & 1)) * 512 + (g & 1) * 256 + c16 * 16 + 8 * (g >> 1);
                __hip_atomic_store((gu64*)dst, o, RLX_AGENT);
            } else {
#pragma unroll
                for (int i = 0; i < 16; ++i) acc = __builtin_amdgcn_mfma_f32_16x16x32_bf16(wf[i], xf[i], acc, 0, 0, 0);
                const int key = 16 * (kg & 1) + c16;
                const int rho = (key & 19) | ((key & 4) << 1) | ((key & 8) >> 1);
                const u32 o0 = pk_bf16(acc[0], acc[1]), o1 = pk_bf16(acc[2], acc[3]);
                char* dst = ws + WS_KF + (size_t)T * 1024 + (rho + 32 * (g & 1)) * 16 + 8 * (g >> 1);
                __hip_atomic_store((gu64*)dst, (u64)o0 | ((u64)o1 << 32), RLX_AGENT);
                float n2 = bf_lo(o0) * bf_lo(o0) + bf_hi(o0) * bf_hi(o0) + bf_lo(o1) * bf_lo(o1) + bf_hi(o1) * bf_hi(o1);
                n2 = add_rows16(n2);
                n2 = add_halves(n2);
                n2 = wave_max(n2);
                if (lane == 0) __hip_atomic_store((gu32*)(ws + WS_KM) + 4 * sb + kg, __builtin_bit_cast(u32, n2), RLX_AGENT);
            }
            DRAIN();
            STAMPR(18);
            if (lane < 8) __hip_atomic_fetch_add(cnt_ptr(ws, C_KV + lane * 8 + (b & 7)), 1u, RLX_AGENT);
        }
        BAR();
        {
            const int row = 16 * cw + (lane >> 2), part = lane & 3;
            const char* xb = smem + L_X + row * 1040 + 16 * part;
            const char* gb = smem + L_GW + 16 * part;
            float s0 = 0.f, s1 = 0.f, s2 = 0.f, s3 = 0.f;
#pragma unroll
            for (int hf = 0; hf < 2; ++hf) {
                u32x4 xv[8], gv[8];
#pragma unroll
                for (int i = 0; i < 8; ++i) { xv[i] = lds_rd128(xb + 64 * (8 * hf + i)); gv[i] = lds_rd128(gb + 64 * (8 * hf + i)); }
                __builtin_amdgcn_sched_barrier(0);
#pragma unroll
                for (int i = 0; i < 8; ++i) {
                    s0 = dot2(xv[i][0], gv[i][0], s0);
                    s1 = dot2(xv[i][1], gv[i][1], s1);
                    s2 = dot2(xv[i][2], gv[i][2], s2);
                    s3 = dot2(xv[i][3], gv[i][3], s3);
                }
            }
            float s = (s0 + s1) + (s2 + s3);
            s += dpp_mov<0xb1>(s, s);
            s += dpp_mov<0x4e>(s, s);
            if (part == 0) ((float*)(smem + L_G))[row] = 1.f / (1.f + __expf(-s));
        }
        STAMP(9);
        BAR();
        {
            const int rl = lane >> 3, cpos = lane & 7;
#pragma unroll
            for (int i = 0; i < 5; ++i) {
                const int pc = cw + 4 * i;
                if (pc < 17) {
                    const bool isv = pc >= 9;
                    const int r = 8 * (isv ? pc - 9 : pc) + rl;
                    int t = 64 * sb + r - (isv ? 0 : 3);
                    t = t < 0 ? 0 : (t > SEQ - 1 ? SEQ - 1 : t);
                    const u16* src = (isv ? vin : kin) + (size_t)t * DM + 64 * hg + 8 * (cpos ^ ((r >> 3) & 7));
                    dma16(src, smem + L_TS + pc * 1024);
                }
            }
        }
        const u16* hrow = ssm_k + (size_t)chg * SEQ;
        u32x4 hn[3];
        u32 hn4[3];
        u32 dv16;
        const int d0 = 64 * hg + 16 * cw + 2 * (lane >> 3);
        u32x4 sw;
        {
            const u32x4 srd = {(u32)(size_t)hrow, (u32)((size_t)hrow >> 32) & 0xffffu, 4096u, 0x00020000u};
#pragma unroll
            for (int p = 0; p < 3; ++p) {
                const int hi = 130 * (cw >> 1) + 64 * p + lane;
                const u32 off = (u32)(16 * (hi - 4));
                asm volatile("buffer_load_dwordx4 %0, %2, %3, 0 offen\n\tbuffer_load_dword %1, %2, %3, 0 offen offset:16"
                             : "=&v"(hn[p]), "=&v"(hn4[p]) : "v"(off), "s"(srd) : "memory");
            }
            asm volatile("global_load_ushort %0, %2, off\n\tglobal_load_dwordx4 %1, %3, off"
                         : "=&v"(dv16), "=&v"(sw) : "v"(ssm_D + chg), "v"(shift_k + d0 * 4) : "memory");
        }
        {
            const int t = tid - 512;
            if (t < 160) *(u32x4*)(smem + L_U + 64 * 80 + t * 16) = u32x4{0, 0, 0, 0};
            if (t >= 152) {
                const int sl = (t - 152) & 31;
                const u32 v = (sl == 0 || sl == 4) ? 0x3f803f80u : 0u;
                *(u32x4*)(smem + L_ONES + (t - 152) * 16) = u32x4{v, v, v, v};
            }
        }
        if (cw == 3) {
            STAMPR(16);
            poll_counters(ws, C_KV + ((b >> 3) & 7) * 8 + (lane & 7), 32u);
            STAMPR(17);
            if (lane == 0) lds_add(smem, L_FLAG + 16);
        }
        STAMP(2);
        asm volatile("s_waitcnt vmcnt(0)"
                     : "+v"(hn[0]), "+v"(hn[1]), "+v"(hn[2]), "+v"(hn4[0]), "+v"(hn4[1]), "+v"(hn4[2]), "+v"(dv16), "+v"(sw)::"memory");
        if (lane == 0) lds_add(smem, L_FLAG + 20);
        lds_wait_ge(smem, L_FLAG + 20, 4u);
        STAMP(3);
        {
            const int tb = lane & 7, dp = lane >> 3;
            const int t0 = 64 * sb + 8 * tb;
            const int j = 2 * cw + (dp >> 2);
            u32 kr[11], vr[8];
#pragma unroll
            for (int i = 0; i < 11; ++i) {
                const int r = 8 * tb + i;
                kr[i] = *(const u32*)(smem + L_TS + r * 128 + 16 * (j ^ ((r >> 3) & 7)) + 4 * (dp & 3));
                if (i < 3 && t0 - 3 + i < 0) kr[i] = 0u;
            }
#pragma unroll
            for (int i = 0; i < 8; ++i) vr[i] = *(const u32*)(smem + L_TS + 9216 + (8 * tb + i) * 128 + 16 * (j ^ tb) + 4 * (dp & 3));
            const float w00 = bf_lo(sw[0]), w01 = bf_hi(sw[0]), w02 = bf_lo(sw[1]), w03 = bf_hi(sw[1]);
            const float w10 = bf_lo(sw[2]), w11 = bf_hi(sw[2]), w12 = bf_lo(sw[3]), w13 = bf_hi(sw[3]);
            float u0[8], u1[8];
#pragma unroll
            for (int i = 0; i < 8; ++i) {
                const float a = bf_lo(kr[i + 3]) * w00 + bf_lo(kr[i + 2]) * w01 + bf_lo(kr[i + 1]) * w02 + bf_lo(kr[i]) * w03;
                const float c = bf_hi(kr[i + 3]) * w10 + bf_hi(kr[i + 2]) * w11 + bf_hi(kr[i + 1]) * w12 + bf_hi(kr[i]) * w13;
                u0[i] = a * bf_lo(vr[i]);
                u1[i] = c * bf_hi(vr[i]);
            }
            const int i0 = 2040 - t0;
            u32x4 o0 = {pk_bf16(u0[7], u0[6]), pk_bf16(u0[5], u0[4]), pk_bf16(u0[3], u0[2]), pk_bf16(u0[1], u0[0])};
            u32x4 o1 = {pk_bf16(u1[7], u1[6]), pk_bf16(u1[5], u1[4]), pk_bf16(u1[3], u1[2]), pk_bf16(u1[1], u1[0])};
            const __amdgpu_buffer_rsrc_t ur = __builtin_amdgcn_make_buffer_rsrc(ws + WS_UR, 0, 2097152, 0x00020000);
            __builtin_amdgcn_raw_buffer_store_b128(o0, ur, (u32)((d0 * SEQ + i0) * 2), 0, 16);
            __builtin_amdgcn_raw_buffer_store_b128(o1, ur, (u32)(((d0 + 1) * SEQ + i0) * 2), 0, 16);
        }
        BAR();
        STAMP(4);
        {
            const char* kg_ = ws + WS_KF + lane * 16;
            const char* vg_ = ws + WS_VF + lane * 16;
#pragma unroll
            for (int i = 0; i < 13; ++i) {
                const int T = 12 + i * 4 + cw;
                dma16_dev(kg_ + T * 1024, smem + L_K + T * 1024);
                dma16_dev(vg_ + T * 1024, smem + L_V + T * 1024);
            }
        }
#pragma unroll
        for (int p = 0; p < 3; ++p) {
            const int hi = 130 * (cw >> 1) + 64 * p + lane;
            if (p < 2 || lane < 2) {
                u32* cE = (u32*)(smem + L_H + ch * 8320) + 4 * hi;
                u32* cO = cE + 1040;
                u32x4 e = {hn[p][1], hn[p][2], hn[p][3], hn4[p]};
                u32x4 o = {__builtin_amdgcn_alignbit(hn[p][1], hn[p][0], 16), __builtin_amdgcn_alignbit(hn[p][2], hn[p][1], 16),
                           __builtin_amdgcn_alignbit(hn[p][3], hn[p][2], 16), __builtin_amdgcn_alignbit(hn4[p], hn[p][3], 16)};
                *(u32x4*)cE = e;
                *(u32x4*)cO = o;
            }
        }
        DRAIN();
        if (lane == 0) lds_add(smem, L_FLAG);
        if (cw == 3) {
            lds_wait_ge(smem, L_FLAG, 4u);
            if (lane < 4) __hip_atomic_fetch_add(cnt_ptr(ws, C_T + hg * 4 + lane), 1u, RLX_AGENT);
            poll_counters(ws, C_T + (b >> 5) * 4 + (b & 3), 32u);
            if (lane == 0) lds_add(smem, L_FLAG + 8);
        }
        lds_wait_ge(smem, L_FLAG + 8, 1u);
        {
            const u16* ug = (const u16*)(ws + WS_UR) + (size_t)chg * SEQ;
#pragma unroll
            for (int i = 0; i < 3; ++i) {
                const int pc = (cw >> 1) ? (i == 2 ? 4 : 3 + i) : i;
                const int z = 64 * pc + lane;
                const int P = z / 5;
                int sg = z - 5 * P;
                if (sg == 4) sg = 0;
                const int beta = ch == 0 ? 63 - P : P;
                dma16_dev(ug + 2016 - 32 * beta + 8 * sg, smem + L_U + (ch == 0 ? 0 : 96 * 80) + pc * 1024);
            }
        }
        asm volatile("s_waitcnt vmcnt(0) lgkmcnt(0)" ::: "memory");
        if (lane == 0) lds_add(smem, L_FLAG + 12);
        lds_wait_ge(smem, L_FLAG + 12, 4u);
        STAMP(5);
        {
            const float Dv = bf_lo(dv16);
            if (ch) conv_wave<1>(smem, cw >> 1, lane, chg, Dv, ws);
            else conv_wave<0>(smem, cw >> 1, lane, chg, Dv, ws);
        }
        DRAIN();
        if (lane == 0) lds_add(smem, L_FLAG + 4);
        STAMP(6);
        __builtin_amdgcn_s_setprio(3);
        if (cw == 3) {
            lds_wait_ge(smem, L_FLAG + 4, 4u);
            if (lane < 4) __hip_atomic_fetch_add(cnt_ptr(ws, C_Y + (b >> 5) * 4 + lane), 1u, RLX_AGENT);
            poll_counters(ws, C_Y + hg * 4 + (sb & 3), 32u);
        }
        STAMPR(13);
        BAR();
        STAMPR(14);
        if (cw == 3) poll_counters(ws, C_O + sb, 8u);
        STAMPR(15);
        STAMP(7);
        BAR();
        BAR();
        BAR();
        STAMP(8); STAMPR(23); STAMP_FLUSH();
        return;
    }

    const int nt = w >> 2, kh = (w >> 1) & 1, st = w & 1;
    const int r31 = lane & 31;
    {
        const int kg = hg >> 1;
        __builtin_amdgcn_s_sleep(4);
        const u16* xg = x + (size_t)(64 * sb) * DM + lane * 8;
#pragma unroll
        for (int i = 0; i < 6; ++i) {
            int row = i * 8 + w + 6 * hg;
            if (row >= 48) row -= 48;
            if (row >= 16 * kg) row += 16;
            dma16(xg + row * DM, smem + L_X + row * 1040);
        }
        const u16* wg = wq + hg * 64;
#pragma unroll
        for (int i = 0; i < 8; ++i) {
            const int k0 = ((i * 8 + w + 16 * (b >> 6)) & 63) * 8;
            const int k = k0 + (lane >> 3);
            const int cp = lane & 7;
            dma16(wg + k * DM + 8 * (cp ^ (4 * ((k >> 1) & 1))), smem + L_W + k0 * 128);
        }
        if (w == 0) dma16(gate_w + lane * 8, smem + L_GW);
    }
    STAMP(1);
    DRAIN();
    STAMP(2);
    BAR();
    STAMP(3);
    const int blk = (lane >> 4) & 1, q4 = (lane & 15) >> 2, p4 = lane & 3;
    f32x4 k0, k1;
    {
        const u32 aaddr = lds0 + L_W + (kh * 256 + 8 * hh + q4) * 128 + 64 * (nt ^ ((q4 >> 1) & 1)) + 32 * blk + 8 * p4;
        const u32 baddr = lds0 + L_X + (32 * st + r31) * 1040 + kh * 512 + 16 * hh;
        const f32x16 acc = gemm_proj(aaddr, baddr);
        STAMP(4);
        exch_write(smem + L_EX1, w, lane, kh, acc, k0, k1);
    }
    BAR();
    u32x4 qf;
    {
        k0 += *(const f32x4*)(smem + L_EX1 + (w ^ 2) * 2048 + lane * 16);
        k1 += *(const f32x4*)(smem + L_EX1 + (w ^ 2) * 2048 + 1024 + lane * 16);
        const float sc = 0.25f * 1.4426950408889634f;
        k0 *= sc;
        k1 *= sc;
        qf = u32x4{pk_bf16(k0[0], k0[1]), pk_bf16(k0[2], k0[3]), pk_bf16(k1[0], k1[1]), pk_bf16(k1[2], k1[3])};
    }
    float q2 = 0.f;
#pragma unroll
    for (int i = 0; i < 4; ++i) q2 += bf_lo(qf[i]) * bf_lo(qf[i]) + bf_hi(qf[i]) * bf_hi(qf[i]);
    q2 = add_halves(q2);
    STAMP(5);
    lds_wait_ge(smem, L_FLAG + 16, 1u);
    STAMP(6);
    {
        const char* kg_ = ws + WS_KF + lane * 16;
        const char* vg_ = ws + WS_VF + lane * 16;
#pragma unroll
        for (int i = 0; i < 3; ++i) {
            const int c = w * 3 + i;
            const int T = c >> 1;
            if (c & 1) dma16_dev(vg_ + T * 1024, smem + L_V + T * 1024);
            else dma16_dev(kg_ + T * 1024, smem + L_K + T * 1024);
        }
    }
    const u64 k2 = __hip_atomic_load((gu64*)(ws + WS_KM) + lane, RLX_AGENT);
    const float km = wave_max(fmaxf(__builtin_bit_cast(float, (u32)k2), __builtin_bit_cast(float, (u32)(k2 >> 32))));
    float mrow = sqrtf(q2 * km) * 1.002f + 1e-6f;
    DRAIN();
    BAR();
    STAMP(7);
    int wait_iter = 2;
    if (!__all(mrow <= 48.f)) {
        lds_wait_ge(smem, L_FLAG, 4u);
        wait_iter = -1;
        float mx = -3.0e38f;
        for (int T = 0; T < 64; ++T) {
            const bf16x8 kf = as_bf8(lds_rd128(smem + L_K + T * 1024 + lane * 16));
            f32x16 s = {0};
            s = __builtin_amdgcn_mfma_f32_32x32x16_bf16(kf, as_bf8(qf), s, 0, 0, 0);
#pragma unroll
            for (int i = 0; i < 16; ++i) mx = fmaxf(mx, s[i]);
        }
        mrow = max_halves(mx);
    }
    wait_iter = __builtin_amdgcn_readfirstlane(wait_iter);
    f32x16 O;
    {
        const bool cst = r31 >= 16;
        const int cslot = r31 == 16 ? 0 : (r31 == 20 ? 4 : ((r31 > 20 && r31 < 28) ? 5 : 1));
        const u32 kaddr = lds0 + L_K + lane * 16;
        const u32 va0 = cst ? (lds0 + L_ONES + cslot * 16) : (lds0 + L_V + hh * 256 + r31 * 16);
        const u32 vinc = cst ? 0u : 4096u;
        const u32 va1 = cst ? va0 : va0 + 2048;
        O = attn_loop(qf, -mrow, kaddr, va0, va1, vinc, lds0 + L_FLAG, wait_iter, (w >> 2) ? PRIO_IT : -1);
    }
    STAMP(8); STAMPR(13);
    {
        const float inv = 1.f / O[8];
        u32x4 o = {pk_bf16(O[0] * inv, O[1] * inv), pk_bf16(O[2] * inv, O[3] * inv), pk_bf16(O[4] * inv, O[5] * inv),
                   pk_bf16(O[6] * inv, O[7] * inv)};
        const int head = 4 * hg + (w >> 1);
        const __amdgpu_buffer_rsrc_t orr = __builtin_amdgcn_make_buffer_rsrc(ws + WS_OF, 0, 2097152, 0x00020000);
        __builtin_amdgcn_raw_buffer_store_b128(o, orr, (u32)(((head * 64 + 2 * sb + st) * 64 + lane) * 16), 0, 16);
    }
    DRAIN();
    BAR();
    STAMP(9);
    if (tid == 0) __hip_atomic_fetch_add(cnt_ptr(ws, C_O + sb), 1u, RLX_AGENT);
    {
        const u16* wg = wo + hg * 64;
#pragma unroll
        for (int i = 0; i < 8; ++i) {
            const int k0_ = (w * 8 + i) * 8;
            const int k = k0_ + (lane >> 3);
            const int cp = lane & 7;
            dma16(wg + k * DM + 8 * (cp ^ (4 * ((k >> 1) & 1))), smem + L_WO + k0_ * 128);
        }
    }
    const int s = 64 * sb + 32 * st + r31;
    const int n0 = 64 * hg + 32 * nt + 16 * kh + 4 * hh;
    u32x2 xa, xb2;
    u32 yv[8];
    {
        const u16* xp = x + (size_t)s * DM + n0;
        asm volatile("global_load_dwordx2 %0, %2, off\n\tglobal_load_dwordx2 %1, %2, off offset:16" : "=&v"(xa), "=&v"(xb2) : "v"(xp) : "memory");
        const u16* yp = (const u16*)(ws + WS_YT) + (size_t)n0 * SEQ + s;
#pragma unroll
        for (int i = 0; i < 8; ++i)
            asm volatile("global_load_ushort %0, %1, off sc1" : "=&v"(yv[i]) : "v"(yp + ((i & 3) + 8 * (i >> 2)) * SEQ) : "memory");
    }
    BAR();
    STAMP(10);
    {
#pragma unroll
        for (int i = 0; i < 8; ++i) {
            const int c = w * 8 + i;
            dma16_dev(ws + WS_OF + ((size_t)((c >> 1) * 64 + 2 * sb + (c & 1)) * 64 + lane) * 16, smem + L_O + c * 1024);
        }
    }
    const float g = ((const float*)(smem + L_G))[32 * st + r31];
    asm volatile("s_waitcnt vmcnt(0)"
                 : "+v"(xa), "+v"(xb2), "+v"(yv[0]), "+v"(yv[1]), "+v"(yv[2]), "+v"(yv[3]), "+v"(yv[4]), "+v"(yv[5]), "+v"(yv[6]), "+v"(yv[7])
                 :: "memory");
    BAR();
    STAMP(11);
    {
        const u32 aaddr = lds0 + L_WO + (kh * 256 + 4 * hh + q4) * 128 + 64 * (nt ^ ((q4 >> 1) & 1)) + 32 * blk + 8 * p4;
        const u32 baddr = lds0 + L_O + (kh * 32 + st) * 1024 + lane * 16;
        const f32x16 acc = gemm_out(aaddr, baddr);
        exch_write(smem + L_EX5, w, lane, kh, acc, k0, k1);
    }
    BAR();
    k0 += *(const f32x4*)(smem + L_EX5 + (w ^ 2) * 2048 + lane * 16);
    k1 += *(const f32x4*)(smem + L_EX5 + (w ^ 2) * 2048 + 1024 + lane * 16);
    const float g1 = 1.f - g;
    float o[8];
    o[0] = g * bf_lo(xa[0]) * bf_lo(yv[0]) + g1 * k0[0];
    o[1] = g * bf_hi(xa[0]) * bf_lo(yv[1]) + g1 * k0[1];
    o[2] = g * bf_lo(xa[1]) * bf_lo(yv[2]) + g1 * k0[2];
    o[3] = g * bf_hi(xa[1]) * bf_lo(yv[3]) + g1 * k0[3];
    o[4] = g * bf_lo(xb2[0]) * bf_lo(yv[4]) + g1 * k1[0];
    o[5] = g * bf_hi(xb2[0]) * bf_lo(yv[5]) + g1 * k1[1];
    o[6] = g * bf_lo(xb2[1]) * bf_lo(yv[6]) + g1 * k1[2];
    o[7] = g * bf_hi(xb2[1]) * bf_lo(yv[7]) + g1 * k1[3];
    const u32 a0 = pk_bf16(o[0], o[1]), a1 = pk_bf16(o[2], o[3]), b0 = pk_bf16(o[4], o[5]), b1 = pk_bf16(o[6], o[7]);
    auto r0 = __builtin_amdgcn_permlane32_swap(a0, b0, false, false);
    auto r1 = __builtin_amdgcn_permlane32_swap(a1, b1, false, false);
    u32x4 ov = {r0[0], r1[0], r0[1], r1[1]};
    *(u32x4*)(out + (size_t)s * DM + 64 * hg + 32 * nt + 16 * kh + 8 * hh) = ov;
    STAMP(12); STAMPR(23); STAMP_FLUSH();
}

extern "C" void kernel_launch(void* const* d_in, const int* in_sizes, int n_in, void* d_out, int out_size, void* d_ws,
                              size_t ws_size, hipStream_t stream) {
    const u16* A_q = (const u16*)d_in[0];
    const u16* A_k = (const u16*)d_in[1];
    const u16* A_v = (const u16*)d_in[2];
    const u16* shift_k = (const u16*)d_in[3];
    const u16* ssm_k = (const u16*)d_in[4];
    const u16* ssm_D = (const u16*)d_in[5];
    const u16* wq = (const u16*)d_in[7];
    const u16* wk = (const u16*)d_in[8];
    const u16* wv = (const u16*)d_in[9];
    const u16* wo = (const u16*)d_in[10];
    const u16* gate_w = (const u16*)d_in[11];
    char* ws = (char*)d_ws;
    (void)hipFuncSetAttribute((const void*)k_mega, hipFuncAttributeMaxDynamicSharedMemorySize, LDS_BYTES);
    k_zero<<<1, 256, 0, stream>>>(ws);
    k_mega<<<256, 768, LDS_BYTES, stream>>>(A_q, A_k, A_v, shift_k, ssm_k, ssm_D, wq, wk, wv, wo, gate_w, ws, (u16*)d_out);
}
```

```cpp
#include <hip/hip_runtime.h>
#include <hip/hip_bf16.h>
#include <stdint.h>

typedef unsigned short u16;
typedef unsigned int u32;
typedef unsigned long long u64;
typedef __attribute__((ext_vector_type(8))) __bf16 bf16x8;
typedef __attribute__((ext_vector_type(4))) short s16x4;
typedef __attribute__((ext_vector_type(16))) float f32x16;
typedef __attribute__((ext_vector_type(4))) float f32x4;
typedef __attribute__((ext_vector_type(4))) u32 u32x4;
typedef __attribute__((ext_vector_type(2))) u32 u32x2;
typedef __attribute__((ext_vector_type(2))) float f32x2;
typedef __attribute__((ext_vector_type(2))) __bf16 bf16x2;
typedef __attribute__((address_space(3))) s16x4* lds_s16x4_p;
typedef __attribute__((address_space(3))) void* lds_void_p;
typedef __attribute__((address_space(3))) u32 lds_u32;
typedef const __attribute__((address_space(1))) void* glb_cvoid_p;
typedef __attribute__((address_space(1))) u32 gu32;
typedef __attribute__((address_space(1))) u64 gu64;

#define DEV __device__ __forceinline__
#define RLX_AGENT __ATOMIC_RELAXED, __HIP_MEMORY_SCOPE_AGENT
#define STR2(x) #x
#define STR(x) STR2(x)

#define STAMP_DECL
#define STAMP(idx)
#define STAMPR(idx)
#define STAMP_FLUSH()

constexpr int SEQ = 2048;
constexpr int DM = 512;

constexpr size_t WS_FL = 0;
constexpr int F_KV = 0;
constexpr int F_T = 2048;
constexpr int F_Y = 3072;
constexpr int F_O = 4096;
constexpr int F_DONE = 6144;
constexpr int F_EPOCH = 6400;
constexpr size_t WS_REQ = 53248;
constexpr size_t WS_ECHO = 65536;
constexpr size_t WS_KF = 98304;
constexpr size_t WS_VF = WS_KF + 65536;
constexpr size_t WS_KM = WS_VF + 65536;
constexpr size_t WS_OF = 262144;
constexpr size_t WS_UR = WS_OF + 2097152;
constexpr size_t WS_YT = WS_UR + 2097152;

constexpr int XP = 1056;
constexpr int L_X = 0;
constexpr int L_W = 67584;
constexpr int L_WKV = 133120;
constexpr int L_TS = 144768;
constexpr int L_K = 0;
constexpr int L_V = 65536;
constexpr int L_ONES = 131072;
constexpr int L_H = L_ONES + 1664;
constexpr int L_U = L_H + 4 * 4160;
constexpr int L_O = 0;
constexpr int L_WO = 65536;
constexpr int L_FLAG = L_U + 160 * 80;
constexpr int L_G = L_FLAG + 48;
constexpr int LDS_BYTES = L_G + 256;
static_assert(LDS_BYTES <= 163840, "lds");
static_assert((L_U & 15) == 0 && (L_H & 15) == 0 && (L_FLAG & 15) == 0, "align");

DEV float bf_lo(u32 v) { return __builtin_bit_cast(float, v << 16); }
DEV float bf_hi(u32 v) { return __builtin_bit_cast(float, v & 0xffff0000u); }
DEV u32 pk_bf16(float a, float b) {
    f32x2 f = {a, b};
    bf16x2 r = __builtin_convertvector(f, bf16x2);
    return __builtin_bit_cast(u32, r);
}
DEV void dma16(const void* g, char* lds) {
    __builtin_amdgcn_global_load_lds((glb_cvoid_p)g, (lds_void_p)lds, 16, 0, 0);
}
DEV void dma16_dev(const void* g, char* lds) {
    __builtin_amdgcn_global_load_lds((glb_cvoid_p)g, (lds_void_p)lds, 16, 0, 16);
}
DEV bf16x8 as_bf8(u32x4 v) { return __builtin_bit_cast(bf16x8, v); }
DEV float dot2(u32 a, u32 b, float c) {
    return __builtin_amdgcn_fdot2_f32_bf16(__builtin_bit_cast(bf16x2, a), __builtin_bit_cast(bf16x2, b), c, false);
}
DEV u32x4 lds_rd128(const char* p) { return *(const u32x4*)p; }
DEV u32x2 lds_tr(const char* p) {
    s16x4 v = __builtin_amdgcn_ds_read_tr16_b64_v4i16((lds_s16x4_p)(p));
    return __builtin_bit_cast(u32x2, v);
}
DEV bf16x8 cat_tr(u32x2 a, u32x2 b) {
    u32x4 r = {a[0], a[1], b[0], b[1]};
    return as_bf8(r);
}
template <int CTRL>
DEV float dpp_mov(float old, float v) {
    return __builtin_bit_cast(float, __builtin_amdgcn_update_dpp(__builtin_bit_cast(int, old), __builtin_bit_cast(int, v), CTRL, 0xf, 0xf, false));
}
DEV float wave_max(float v) {
    v = fmaxf(v, dpp_mov<0x111>(v, v));
    v = fmaxf(v, dpp_mov<0x112>(v, v));
    v = fmaxf(v, dpp_mov<0x114>(v, v));
    v = fmaxf(v, dpp_mov<0x118>(v, v));
    v = fmaxf(v, dpp_mov<0x142>(v, v));
    v = fmaxf(v, dpp_mov<0x143>(v, v));
    return __builtin_bit_cast(float, __builtin_amdgcn_readlane(__builtin_bit_cast(int, v), 63));
}
DEV float add_halves(float v) {
    const u32 x = __builtin_bit_cast(u32, v);
    auto r = __builtin_amdgcn_permlane32_swap(x, x, false, false);
    return __builtin_bit_cast(float, (u32)r[0]) + __builtin_bit_cast(float, (u32)r[1]);
}
DEV float add_rows16(float v) {
    const u32 x = __builtin_bit_cast(u32, v);
    auto r = __builtin_amdgcn_permlane16_swap(x, x, false, false);
    return __builtin_bit_cast(float, (u32)r[0]) + __builtin_bit_cast(float, (u32)r[1]);
}
DEV float max_halves(float v) {
    const u32 x = __builtin_bit_cast(u32, v);
    auto r = __builtin_amdgcn_permlane32_swap(x, x, false, false);
    return fmaxf(__builtin_bit_cast(float, (u32)r[0]), __builtin_bit_cast(float, (u32)r[1]));
}
#define POLL_DELAY 24
#define HEAD_START 16
#define POLL_SLEEP 2
#define BAR() asm volatile("s_waitcnt lgkmcnt(0)\n\ts_barrier" ::: "memory")
#define DRAIN() asm volatile("s_waitcnt vmcnt(0)" ::: "memory")

constexpr u32 SPIN_CAP = 1u << 17;
DEV gu64* flag_ptr(char* ws, int word) { return (gu64*)(ws + WS_FL) + word; }
DEV void flag_set(char* ws, int word, u64 e) { __hip_atomic_store(flag_ptr(ws, word), e, RLX_AGENT); }
template <int N>
DEV void poll_flags(char* ws, int word, u64 e) {
    gu64* p = flag_ptr(ws, word);
    const u32 lo = (u32)e, hi = (u32)(e >> 32);
    bool ok = false;
#pragma nounroll
    for (u32 spins = 0; spins < SPIN_CAP; ++spins) {
        if (!ok) {
            if constexpr (N == 4) {
                u32x4 v0, v1;
                asm volatile("global_load_dwordx4 %0, %2, off sc1\n\tglobal_load_dwordx4 %1, %2, off offset:16 sc1\n\ts_waitcnt vmcnt(0)"
                             : "=&v"(v0), "=&v"(v1) : "v"(p) : "memory");
                ok = v0[0] == lo && v0[1] == hi && v0[2] == lo && v0[3] == hi && v1[0] == lo && v1[1] == hi && v1[2] == lo && v1[3] == hi;
            } else {
                ok = __hip_atomic_load(p, RLX_AGENT) == e;
            }
        }
        if (__all(ok)) break;
        __builtin_amdgcn_s_sleep(POLL_SLEEP);
    }
    __builtin_amdgcn_fence(__ATOMIC_ACQUIRE, "wavefront");
    asm volatile("" ::: "memory");
}

DEV u64 start_up_master(char* ws, int lane) {
    const u64 e = __hip_atomic_load(flag_ptr(ws, F_EPOCH), RLX_AGENT) + 1ull;
    if (lane == 0) {
        __hip_atomic_store(flag_ptr(ws, F_EPOCH), e, RLX_AGENT);
        __hip_atomic_store(flag_ptr(ws, F_DONE), e, RLX_AGENT);
    }
    return e;
}
DEV void start_up_echo(char* ws, int lane, u64 e) {
    const u32 lo = (u32)e, hi = (u32)(e >> 32);
    const __amdgpu_buffer_rsrc_t er = __builtin_amdgcn_make_buffer_rsrc(ws + WS_ECHO, 0, 32768, 0x00020000);
    const char* req = ws + WS_REQ + lane * 16;
    gu64* done = flag_ptr(ws, F_DONE + lane * 4);
    u32x4 last = {0, 0, 0, 0};
    bool first = true;
#pragma nounroll
    for (u32 spins = 0; spins < SPIN_CAP; ++spins) {
        u32x4 r, d0, d1;
        asm volatile("global_load_dwordx4 %0, %3, off sc1\n\tglobal_load_dwordx4 %1, %4, off sc1\n\t"
                     "global_load_dwordx4 %2, %4, off offset:16 sc1\n\ts_waitcnt vmcnt(0)"
                     : "=&v"(r), "=&v"(d0), "=&v"(d1) : "v"(req), "v"(done) : "memory");
        const bool dn[4] = {d0[0] == lo && d0[1] == hi, d0[2] == lo && d0[3] == hi, d1[0] == lo && d1[1] == hi, d1[2] == lo && d1[3] == hi};
#pragma unroll
        for (int j = 0; j < 4; ++j)
            if (!dn[j] && (first || r[j] != last[j]))
                __builtin_amdgcn_raw_buffer_store_b128(u32x4{r[j], lo, r[j], hi}, er, (u32)((lane * 4 + j) * 128), 0, 16);
        last = r;
        first = false;
        if (__all(dn[0] && dn[1] && dn[2] && dn[3])) break;
    }
}
DEV u64 start_up(char* ws, int b) {
    const char* echo = ws + WS_ECHO + (size_t)b * 128;
    const u32 n = __hip_atomic_load((gu32*)echo, RLX_AGENT) + 1u;
    __hip_atomic_store((gu32*)(ws + WS_REQ) + b, n, RLX_AGENT);
    u32x4 v = {0, 0, 0, 0};
#pragma nounroll
    for (u32 spins = 0; spins < SPIN_CAP; ++spins) {
        asm volatile("global_load_dwordx4 %0, %1, off sc1\n\ts_waitcnt vmcnt(0)" : "=&v"(v) : "v"(echo) : "memory");
        if (v[0] == n && v[2] == n) break;
        __builtin_amdgcn_s_sleep(1);
    }
    const u64 e = (u64)v[1] | ((u64)v[3] << 32);
    __hip_atomic_store(flag_ptr(ws, F_DONE + b), e, RLX_AGENT);
    return e;
}
DEV void lds_wait_ge(char* smem, int off, u32 want) {
    volatile lds_u32* f = (volatile lds_u32*)(lds_void_p)(smem + off);
#pragma nounroll
    for (u32 spins = 0; spins < (SPIN_CAP << 4); ++spins) {
        if (*f >= want) break;
        __builtin_amdgcn_s_sleep(1);
    }
    asm volatile("" ::: "memory");
}
DEV u64 lds_epoch(char* smem) {
    volatile lds_u32* f = (volatile lds_u32*)(lds_void_p)(smem + L_FLAG + 32);
    return (u64)f[0] | ((u64)f[1] << 32);
}
DEV void lds_add(char* smem, int off) {
    __hip_atomic_fetch_add((lds_u32*)(lds_void_p)(smem + off), 1u, __ATOMIC_RELAXED, __HIP_MEMORY_SCOPE_WORKGROUP);
}

#define GM_RD(A0, A1, B0, B1, I, TOFF, BSTEP, BOFF)                        \
    "ds_read_b64_tr_b16 " A0 ", v80 offset:" #I "*4096\n\t"                \
    "ds_read_b64_tr_b16 " A1 ", v80 offset:" #I "*4096+" TOFF "\n\t"       \
    "ds_read_b128 " B0 ", v81 offset:" #I "*" BSTEP "\n\t"                 \
    "ds_read_b128 " B1 ", v81 offset:" #I "*" BSTEP "+" BOFF "\n\t"
#define GM_R0(I, T, S, O) GM_RD("v[32:33]", "v[34:35]", "v[36:39]", "v[40:43]", I, T, S, O)
#define GM_R1(I, T, S, O) GM_RD("v[44:45]", "v[46:47]", "v[48:51]", "v[52:55]", I, T, S, O)
#define GM_R2(I, T, S, O) GM_RD("v[56:57]", "v[58:59]", "v[60:63]", "v[64:67]", I, T, S, O)
#define GM_R3(I, T, S, O) GM_RD("v[68:69]", "v[70:71]", "v[72:75]", "v[76:79]", I, T, S, O)
#define GM_M(W, A, B0, B1, C0, C1)                                                     \
    "s_waitcnt lgkmcnt(" #W ")\n\t"                                                    \
    "v_mfma_f32_16x16x32_bf16 v[16:19], " A ", " B0 ", " C0 "\n\t"                     \
    "v_mfma_f32_16x16x32_bf16 v[20:23], " A ", " B1 ", " C1 "\n\t"
#define GM_M0(W, C0, C1) GM_M(W, "v[32:35]", "v[36:39]", "v[40:43]", C0, C1)
#define GM_M1(W) GM_M(W, "v[44:47]", "v[48:51]", "v[52:55]", "v[16:19]", "v[20:23]")
#define GM_M2(W) GM_M(W, "v[56:59]", "v[60:63]", "v[64:67]", "v[16:19]", "v[20:23]")
#define GM_M3(W) GM_M(W, "v[68:71]", "v[72:75]", "v[76:79]", "v[16:19]", "v[20:23]")
#define GM_M0A(W) GM_M0(W, "v[16:19]", "v[20:23]")
#define GEMM16(T, S, O)                                                                              \
    GM_R0(0, T, S, O) GM_R1(1, T, S, O) GM_R2(2, T, S, O)                                            \
    GM_R3(3, T, S, O) GM_M0(12, "0", "0") GM_R0(4, T, S, O) GM_M1(12) GM_R1(5, T, S, O) GM_M2(12)    \
    GM_R2(6, T, S, O) GM_M3(12) GM_R3(7, T, S, O) GM_M0A(12) GM_R0(8, T, S, O) GM_M1(12)             \
    GM_R1(9, T, S, O) GM_M2(12) GM_R2(10, T, S, O) GM_M3(12) GM_R3(11, T, S, O) GM_M0A(12)           \
    GM_R0(12, T, S, O) GM_M1(12) GM_R1(13, T, S, O) GM_M2(12) GM_R2(14, T, S, O) GM_M3(12)           \
    GM_R3(15, T, S, O) GM_M0A(12) GM_M1(8) GM_M2(4) GM_M3(0)                                         \
    "s_nop 7\n\t"
#define GEMM_CLOB                                                                                        \
    "memory", "v32", "v33", "v34", "v35", "v36", "v37", "v38", "v39", "v40", "v41", "v42", "v43", "v44", \
    "v45", "v46", "v47", "v48", "v49", "v50", "v51", "v52", "v53", "v54", "v55", "v56", "v57", "v58",   \
    "v59", "v60", "v61", "v62", "v63", "v64", "v65", "v66", "v67", "v68", "v69", "v70", "v71", "v72",   \
    "v73", "v74", "v75", "v76", "v77", "v78", "v79"
DEV void gemm_proj(u32 aaddr, u32 baddr, f32x4& c0, f32x4& c1) {
    asm volatile(GEMM16("512", "64", "16896") : "=&{v[16:19]}"(c0), "=&{v[20:23]}"(c1) : "{v80}"(aaddr), "{v81}"(baddr) : GEMM_CLOB);
}
DEV void gemm_out(u32 aaddr, u32 baddr, f32x4& c0, f32x4& c1) {
    asm volatile(GEMM16("1024", "4096", "256") : "=&{v[16:19]}"(c0), "=&{v[20:23]}"(c1) : "{v80}"(aaddr), "{v81}"(baddr) : GEMM_CLOB);
}

#define CV_RD(SET_A0, SET_A1, SET_B, MI, UOFF)                                                    \
    "ds_read2_b32 " SET_A0 ", v64 offset0:" #MI "*8 offset1:" #MI "*8+1\n\t"                      \
    "ds_read2_b32 " SET_A1 ", v64 offset0:" #MI "*8+2 offset1:" #MI "*8+3\n\t"                    \
    "ds_read_b128 " SET_B ", v65 offset:" UOFF "\n\t"
#define CV_RD0(MI, UOFF) CV_RD("v[32:33]", "v[34:35]", "v[36:39]", MI, UOFF)
#define CV_RD1(MI, UOFF) CV_RD("v[40:41]", "v[42:43]", "v[44:47]", MI, UOFF)
#define CV_RD2(MI, UOFF) CV_RD("v[48:49]", "v[50:51]", "v[52:55]", MI, UOFF)
#define CV_RD3(MI, UOFF) CV_RD("v[56:57]", "v[58:59]", "v[60:63]", MI, UOFF)
#define CV_MM(A, B) "s_waitcnt lgkmcnt(9)\n\tv_mfma_f32_32x32x16_bf16 v[16:31], " A ", " B ", v[16:31]\n\t" CV_PACE
#define CV_MM0 CV_MM("v[32:35]", "v[36:39]")
#define CV_MM1 CV_MM("v[40:43]", "v[44:47]")
#define CV_MM2 CV_MM("v[48:51]", "v[52:55]")
#define CV_MM3 CV_MM("v[56:59]", "v[60:63]")
#define CONV_SLEEP 0
#define CONV_PRIO 0
#define CV_PACE ""
#define CV_BODY(UO)                                                        \
    CV_RD3(2, UO(1) "+0")  CV_MM0                                           \
    CV_RD0(5, UO(2) "+32") CV_MM1                                           \
    CV_RD1(4, UO(2) "+0")  CV_MM2                                           \
    CV_RD2(7, UO(3) "+32") CV_MM3                                           \
    CV_RD3(6, UO(3) "+0")  CV_MM0                                           \
    CV_RD0(9, UO(4) "+32") CV_MM1                                           \
    CV_RD1(8, UO(4) "+0")  CV_MM2                                           \
    CV_RD2(11, UO(5) "+32") CV_MM3                                          \
    CV_RD3(10, UO(5) "+0") CV_MM0                                           \
    CV_RD0(13, UO(6) "+32") CV_MM1                                          \
    CV_RD1(12, UO(6) "+0") CV_MM2                                           \
    CV_RD2(15, UO(7) "+32") CV_MM3                                          \
    CV_RD3(14, UO(7) "+0") CV_MM0                                           \
    CV_RD0(17, UO(8) "+32") CV_MM1                                          \
    CV_RD1(16, UO(8) "+0") CV_MM2                                           \
    CV_RD2(19, UO(9) "+32") CV_MM3
#define CV_UO0(dd) "80*" #dd
#define CV_UO1(dd) "80*(9-" #dd ")"
#define CV_ASM(UO, USTEP)                                                                          \
    asm volatile(                                                                                  \
        "s_setprio " STR(CONV_PRIO) "\n\t"                                                         \
        "v_mov_b32 v16, 0\n\t v_mov_b32 v17, 0\n\t v_mov_b32 v18, 0\n\t v_mov_b32 v19, 0\n\t"     \
        "v_mov_b32 v20, 0\n\t v_mov_b32 v21, 0\n\t v_mov_b32 v22, 0\n\t v_mov_b32 v23, 0\n\t"     \
        "v_mov_b32 v24, 0\n\t v_mov_b32 v25, 0\n\t v_mov_b32 v26, 0\n\t v_mov_b32 v27, 0\n\t"     \
        "v_mov_b32 v28, 0\n\t v_mov_b32 v29, 0\n\t v_mov_b32 v30, 0\n\t v_mov_b32 v31, 0\n\t"     \
        CV_RD0(1, UO(0) "+32") CV_RD1(0, UO(0) "+0") CV_RD2(3, UO(1) "+32")                        \
        "s_mov_b32 s41, %[n]\n\t"                                                                  \
        "L_cv_%=:\n\t"                                                                             \
        CV_BODY(UO)                                                                                \
        "v_add_u32 v64, 0x200, v64\n\t"                                                            \
        "v_add_u32 v65, " USTEP ", v65\n\t"                                                        \
        "s_sub_u32 s41, s41, 1\n\t"                                                                \
        "s_cmp_lg_u32 s41, 0\n\t"                                                                  \
        "s_cbranch_scc1 L_cv_%=\n\t"                                                               \
        "s_waitcnt lgkmcnt(0)\n\t"                                                                 \
        "s_setprio 1\n\t"                                                                          \
        "s_nop 15\n\t"                                                                             \
        : "=&{v[16:31]}"(acc), "+{v64}"(ha), "+{v65}"(ua)                                          \
        : [n] "s"(nit)                                                                             \
        : "memory", "scc", "s41", "v32", "v33", "v34", "v35", "v36", "v37", "v38", "v39", "v40", "v41", "v42", "v43", \
          "v44", "v45", "v46", "v47", "v48", "v49", "v50", "v51", "v52", "v53", "v54", "v55", "v56", "v57", "v58", "v59", \
          "v60", "v61", "v62", "v63")

template <int CH>
DEV void conv_wave(char* smem, int nt, int lane, int ch_global, float Dv, char* __restrict__ ws) {
    const int r = lane & 31, hh = lane >> 5;
    const u32 lds0 = (u32)(size_t)(lds_void_p)smem;
    u32 ha = lds0 + L_H + CH * 8320 + ((r & 1) ? 0 : 4160) + 16 * hh + 4 * (r >> 1);
    u32 ua;
    if (CH == 0) ua = lds0 + L_U + (63 - 32 * nt - r) * 80 + 16 * hh;
    else ua = lds0 + L_U + 96 * 80 + (32 * nt + r - 9) * 80 + 16 * hh;
    const int nit = __builtin_amdgcn_readfirstlane(4 * (nt + 1));
    f32x16 acc;
    if (CH == 0) CV_ASM(CV_UO0, "0x280");
    else CV_ASM(CV_UO1, "0xfffffd80");
    const int beta = 32 * nt + r;
    const char* ublk = (CH == 0) ? (smem + L_U + (63 - beta) * 80) : (smem + L_U + 96 * 80 + beta * 80);
    u32 pk[8];
#pragma unroll
    for (int qd = 0; qd < 4; ++qd) {
        const u32x2 uu = *(const u32x2*)(ublk + 2 * (28 - 8 * qd - 4 * hh));
        pk[2 * qd] = pk_bf16(acc[4 * qd + 0] + Dv * bf_hi(uu[1]), acc[4 * qd + 1] + Dv * bf_lo(uu[1]));
        pk[2 * qd + 1] = pk_bf16(acc[4 * qd + 2] + Dv * bf_hi(uu[0]), acc[4 * qd + 3] + Dv * bf_lo(uu[0]));
    }
    const __amdgpu_buffer_rsrc_t yr = __builtin_amdgcn_make_buffer_rsrc(ws + WS_YT, 0, 2097152, 0x00020000);
#pragma unroll
    for (int pr = 0; pr < 2; ++pr) {
        auto r0 = __builtin_amdgcn_permlane32_swap(pk[4 * pr], pk[4 * pr + 2], false, false);
        auto r1 = __builtin_amdgcn_permlane32_swap(pk[4 * pr + 1], pk[4 * pr + 3], false, false);
        u32x4 ov = {r0[0], r1[0], r0[1], r1[1]};
        __builtin_amdgcn_raw_buffer_store_b128(ov, yr, (u32)((ch_global * SEQ + 32 * beta + 16 * pr + 8 * hh) * 2), 0, 16);
    }
}

#define E_(r) "v_exp_f32 v" #r ", v" #r "\n\t"
#define TILE(C0, C1, C2, C3, C4, C5, C6, C7, C8, C9, C10, C11, C12, C13, C14, C15, CLO, NHI, NALL, VH_ADDR, KOFF, VL_ADDR, EXTRA) \
    E_(C0) E_(C1)                                                                                   \
    "s_waitcnt lgkmcnt(2)\n\t"                                                                      \
    "v_smfmac_f32_32x32x32_bf16 v[64:79], v[108:111], " NHI ", v112\n\t"                            \
    "ds_read_b128 v[108:111], " VH_ADDR "\n\t"                                                      \
    E_(C2) E_(C3) E_(C4)                                                                            \
    "s_waitcnt lgkmcnt(2)\n\t"                                                                      \
    "v_mfma_f32_32x32x16_bf16 " NALL ", v[100:103], v[96:99], v[80:95]\n\t"                         \
    "ds_read_b128 v[100:103], v113 offset:" KOFF "\n\t"                                             \
    E_(C5) E_(C6) E_(C7) E_(C8) EXTRA E_(C9) E_(C10)                                                \
    "s_waitcnt lgkmcnt(2)\n\t"                                                                      \
    "v_smfmac_f32_32x32x32_bf16 v[64:79], v[104:107], " CLO ", v112\n\t"                            \
    "ds_read_b128 v[104:107], " VL_ADDR "\n\t"                                                      \
    E_(C11) E_(C12) E_(C13) E_(C14) E_(C15)
#define TILE_A(VH_ADDR, KOFF, VL_ADDR, EXTRA) \
    TILE(32, 33, 34, 35, 36, 37, 38, 39, 40, 41, 42, 43, 44, 45, 46, 47, "v[32:39]", "v[56:63]", "v[48:63]", VH_ADDR, KOFF, VL_ADDR, EXTRA)
#define TILE_B(VH_ADDR, KOFF, VL_ADDR, EXTRA) \
    TILE(48, 49, 50, 51, 52, 53, 54, 55, 56, 57, 58, 59, 60, 61, 62, 63, "v[48:55]", "v[40:47]", "v[32:47]", VH_ADDR, KOFF, VL_ADDR, EXTRA)
#define PRIO_IT 5

DEV f32x16 attn_loop(u32x4 qf, float negm, u32 kaddr, u32 va0, u32 va1, u32 vinc, u32 flagaddr, int wait_iter, int prio_iter) {
    f32x16 O;
    asm volatile(
        "v_mov_b32 v80, %[negm]\n\t v_mov_b32 v81, %[negm]\n\t v_mov_b32 v82, %[negm]\n\t v_mov_b32 v83, %[negm]\n\t"
        "v_mov_b32 v84, %[negm]\n\t v_mov_b32 v85, %[negm]\n\t v_mov_b32 v86, %[negm]\n\t v_mov_b32 v87, %[negm]\n\t"
        "v_mov_b32 v88, %[negm]\n\t v_mov_b32 v89, %[negm]\n\t v_mov_b32 v90, %[negm]\n\t v_mov_b32 v91, %[negm]\n\t"
        "v_mov_b32 v92, %[negm]\n\t v_mov_b32 v93, %[negm]\n\t v_mov_b32 v94, %[negm]\n\t v_mov_b32 v95, %[negm]\n\t"
        "ds_read_b128 v[100:103], v113\n\t"
        "v_mov_b32 v112, 0xdddd\n\t"
        "v_mov_b32 v64, 0\n\t v_mov_b32 v65, 0\n\t v_mov_b32 v66, 0\n\t v_mov_b32 v67, 0\n\t"
        "v_mov_b32 v68, 0\n\t v_mov_b32 v69, 0\n\t v_mov_b32 v70, 0\n\t v_mov_b32 v71, 0\n\t"
        "v_mov_b32 v72, 0\n\t v_mov_b32 v73, 0\n\t v_mov_b32 v74, 0\n\t v_mov_b32 v75, 0\n\t"
        "v_mov_b32 v76, 0\n\t v_mov_b32 v77, 0\n\t v_mov_b32 v78, 0\n\t v_mov_b32 v79, 0\n\t"
        "v_mov_b32 v56, 0\n\t v_mov_b32 v57, 0\n\t v_mov_b32 v58, 0\n\t v_mov_b32 v59, 0\n\t"
        "v_mov_b32 v60, 0\n\t v_mov_b32 v61, 0\n\t v_mov_b32 v62, 0\n\t v_mov_b32 v63, 0\n\t"
        "v_mov_b32 v108, 0\n\t v_mov_b32 v109, 0\n\t v_mov_b32 v110, 0\n\t v_mov_b32 v111, 0\n\t"
        "s_waitcnt lgkmcnt(0)\n\t"
        "v_mfma_f32_32x32x16_bf16 v[32:47], v[100:103], v[96:99], v[80:95]\n\t"
        "s_nop 7\n\t"
        "ds_read_b128 v[100:103], v113 offset:1024\n\t"
        "ds_read_b128 v[104:107], v114\n\t"
        "s_mov_b32 s40, 0\n\t"
        "s_nop 7\n\t"
        "s_waitcnt lgkmcnt(0)\n\t"
        "L_loop_%=:\n\t"
        "s_cmp_lg_u32 s40, %[wit]\n\t"
        "s_cbranch_scc1 L_nowait_%=\n\t"
        "s_mov_b32 s42, 0x200000\n\t"
        "L_spin_%=:\n\t"
        "ds_read_b32 v117, %[flag]\n\t"
        "s_waitcnt lgkmcnt(0)\n\t"
        "v_readfirstlane_b32 s41, v117\n\t"
        "s_sub_u32 s42, s42, 1\n\t"
        "s_cbranch_scc1 L_nowait_%=\n\t"
        "s_nop 1\n\t"
        "s_cmp_ge_u32 s41, 4\n\t"
        "s_cbranch_scc1 L_nowait_%=\n\t"
        "s_sleep 1\n\t"
        "s_branch L_spin_%=\n\t"
        "L_nowait_%=:\n\t"
        "s_cmp_lg_u32 s40, %[pit]\n\t"
        "s_cbranch_scc1 L_noprio_%=\n\t"
        "s_setprio 1\n\t"
        "L_noprio_%=:\n\t"
        TILE_A("v114 offset:512", "2048", "v114 offset:1024", "")
        TILE_B("v114 offset:1536", "3072", "v115", "")
        TILE_A("v115 offset:512", "4096", "v115 offset:1024", "v_add_u32 v114, v114, v116\n\t")
        TILE_B("v115 offset:1536", "5120", "v114", "")
        "v_add_u32 v113, 0x1000, v113\n\t"
        "v_add_u32 v115, v115, v116\n\t"
        "s_add_u32 s40, s40, 1\n\t"
        "s_cmp_lt_u32 s40, 16\n\t"
        "s_cbranch_scc1 L_loop_%=\n\t"
        "s_waitcnt lgkmcnt(0)\n\t"
        "v_smfmac_f32_32x32x32_bf16 v[64:79], v[108:111], v[56:63], v112\n\t"
        "s_setprio 0\n\t"
        "s_nop 15\n\t"
        "s_nop 7\n\t"
        : "=&{v[64:79]}"(O), "+{v113}"(kaddr), "+{v114}"(va0), "+{v115}"(va1)
        : [negm] "v"(negm), [wit] "s"(wait_iter), [pit] "s"(prio_iter), [flag] "v"(flagaddr), "{v[96:99]}"(qf), "{v116}"(vinc)
        : "memory", "scc", "s40", "s41", "s42", "v32", "v33", "v34", "v35", "v36", "v37", "v38", "v39", "v40", "v41", "v42", "v43", "v44",
          "v45", "v46", "v47", "v48", "v49", "v50", "v51", "v52", "v53", "v54", "v55", "v56", "v57", "v58", "v59", "v60", "v61",
          "v62", "v63", "v80", "v81", "v82", "v83", "v84", "v85", "v86", "v87", "v88", "v89", "v90", "v91", "v92", "v93",
          "v94", "v95", "v100", "v101", "v102", "v103", "v104", "v105", "v106", "v107", "v108", "v109", "v110", "v111", "v112", "v117");
    return O;
}

__global__ __launch_bounds__(768, 3) void k_mega(const u16* __restrict__ x, const u16* __restrict__ kin,
                                                 const u16* __restrict__ vin, const u16* __restrict__ shift_k,
                                                 const u16* __restrict__ ssm_k, const u16* __restrict__ ssm_D,
                                                 const u16* __restrict__ wq, const u16* __restrict__ wk,
                                                 const u16* __restrict__ wv, const u16* __restrict__ wo,
                                                 const u16* __restrict__ gate_w, char* __restrict__ ws,
                                                 u16* __restrict__ out) {
    extern __shared__ __attribute__((aligned(16))) char smem[];
    const int b = blockIdx.x;
    const int sb = (b & 7) * 4 + (b >> 6);
    const int hg = (b >> 3) & 7;
    const int tid = threadIdx.x;
    const int lane = tid & 63;
    const int w = __builtin_amdgcn_readfirstlane(tid >> 6);
    const int hh = lane >> 5;
    const u32 lds0 = (u32)(size_t)(lds_void_p)smem;
    STAMP_DECL STAMPR(22); STAMP(0);
    if (tid < 3) *(u32x4*)(smem + L_FLAG + 16 * tid) = u32x4{0, 0, 0, 0};
    BAR();

    if (w >= 8) {
        const int cw = w - 8, ch = cw & 1;
        const int chg = 2 * b + ch;
        __builtin_amdgcn_s_setprio(3);
        const int kg = hg >> 1;
        if (cw == 0) {
            const u16* wkv = (hg & 1) ? wv : wk;
#pragma unroll
            for (int i = 0; i < 16; ++i) dma16(wkv + i * 512 + lane * 8, smem + L_WKV + i * 1024);
            const u16* xg = x + (size_t)(64 * sb + 16 * kg) * DM + lane * 8;
#pragma unroll
            for (int i = 0; i < 16; ++i) dma16(xg + i * DM, smem + L_X + (16 * kg + i) * XP);
            DRAIN();
            if (lane == 0) lds_add(smem, L_FLAG + 16);
            STAMP(1);
            const int g = lane >> 4, c16 = lane & 15;
            const int q = c16 >> 2, p = lane & 3;
            const char* xb = smem + L_X + (16 * kg + c16) * XP + 16 * g;
            const char* wb = smem + L_WKV + (8 * g + q) * 32 + 8 * p;
            bf16x8 xf[16], wf[16];
#pragma unroll
            for (int i = 0; i < 16; ++i) {
                xf[i] = as_bf8(lds_rd128(xb + i * 64));
                wf[i] = cat_tr(lds_tr(wb + i * 1024), lds_tr(wb + i * 1024 + 128));
            }
            __builtin_amdgcn_sched_barrier(0);
            asm volatile("s_waitcnt lgkmcnt(0)" : "+v"(xf[15]), "+v"(wf[15])::"memory");
            if (lane == 0) lds_add(smem, L_FLAG + 20);
            f32x4 acc = {0.f, 0.f, 0.f, 0.f};
            const int T = 2 * sb + (kg >> 1);
            if (hg & 1) {
#pragma unroll
                for (int i = 0; i < 16; ++i) acc = __builtin_amdgcn_mfma_f32_16x16x32_bf16(xf[i], wf[i], acc, 0, 0, 0);
                const u64 o = (u64)pk_bf16(acc[0], acc[1]) | ((u64)pk_bf16(acc[2], acc[3]) << 32);
                char* dst = ws + WS_VF + (size_t)(T * 2 + (kg & 1)) * 512 + (g & 1) * 256 + c16 * 16 + 8 * (g >> 1);
                __hip_atomic_store((gu64*)dst, o, RLX_AGENT);
            } else {
#pragma unroll
                for (int i = 0; i < 16; ++i) acc = __builtin_amdgcn_mfma_f32_16x16x32_bf16(wf[i], xf[i], acc, 0, 0, 0);
                const int key = 16 * (kg & 1) + c16;
                const int rho = (key & 19) | ((key & 4) << 1) | ((key & 8) >> 1);
                const u32 o0 = pk_bf16(acc[0], acc[1]), o1 = pk_bf16(acc[2], acc[3]);
                char* dst = ws + WS_KF + (size_t)T * 1024 + (rho + 32 * (g & 1)) * 16 + 8 * (g >> 1);
                __hip_atomic_store((gu64*)dst, (u64)o0 | ((u64)o1 << 32), RLX_AGENT);
                float n2 = bf_lo(o0) * bf_lo(o0) + bf_hi(o0) * bf_hi(o0) + bf_lo(o1) * bf_lo(o1) + bf_hi(o1) * bf_hi(o1);
                n2 = add_rows16(n2);
                n2 = add_halves(n2);
                n2 = wave_max(n2);
                if (lane == 0) __hip_atomic_store((gu32*)(ws + WS_KM) + 4 * sb + kg, __builtin_bit_cast(u32, n2), RLX_AGENT);
            }
            DRAIN();
            lds_wait_ge(smem, L_FLAG + 28, 1u);
            STAMPR(18);
            if (lane < 8) flag_set(ws, F_KV + lane * 256 + b, lds_epoch(smem));
        }
        if (cw == 1 || cw == 2) {
            u32x4 gwf[16];
            {
                const u16* gp = gate_w + 8 * (lane >> 4);
#pragma unroll
                for (int i = 0; i < 16; ++i) asm volatile("global_load_dwordx4 %0, %1, off offset:%2" : "=&v"(gwf[i]) : "v"(gp), "i"(64 * i) : "memory");
            }
            lds_wait_ge(smem, L_FLAG + 20, 1u);
            const int rl = lane >> 3, cpos = lane & 7;
#pragma unroll
            for (int i = 0; i < 9; ++i) {
                int pc = (cw - 1) + 2 * i;
                if (pc > 16) pc = 16;
                {
                    const bool isv = pc >= 9;
                    const int r = 8 * (isv ? pc - 9 : pc) + rl;
                    int t = 64 * sb + r - (isv ? 0 : 3);
                    t = t < 0 ? 0 : (t > SEQ - 1 ? SEQ - 1 : t);
                    const u16* src = (isv ? vin : kin) + (size_t)t * DM + 64 * hg + 8 * (cpos ^ ((r >> 3) & 7));
                    dma16(src, smem + L_TS + pc * 1024);
                }
            }
            asm volatile("s_waitcnt vmcnt(9)"
                         : "+v"(gwf[0]), "+v"(gwf[1]), "+v"(gwf[2]), "+v"(gwf[3]), "+v"(gwf[4]), "+v"(gwf[5]), "+v"(gwf[6]), "+v"(gwf[7]),
                           "+v"(gwf[8]), "+v"(gwf[9]), "+v"(gwf[10]), "+v"(gwf[11]), "+v"(gwf[12]), "+v"(gwf[13]), "+v"(gwf[14]), "+v"(gwf[15])
                         :: "memory");
            lds_wait_ge(smem, L_FLAG + 16, 9u);
            const int g = lane >> 4, c16 = lane & 15;
#pragma unroll
            for (int j = 0; j < 2; ++j) {
                const int row0 = 32 * (cw - 1) + 16 * j;
                const char* xb = smem + L_X + (row0 + c16) * XP + 16 * g;
                bf16x8 xf[16];
#pragma unroll
                for (int i = 0; i < 16; ++i) xf[i] = as_bf8(lds_rd128(xb + 64 * i));
                __builtin_amdgcn_sched_barrier(0);
                f32x4 acc = {0.f, 0.f, 0.f, 0.f};
#pragma unroll
                for (int i = 0; i < 16; ++i) acc = __builtin_amdgcn_mfma_f32_16x16x32_bf16(as_bf8(gwf[i]), xf[i], acc, 0, 0, 0);
                if (lane < 16) ((float*)(smem + L_G))[row0 + lane] = 1.f / (1.f + __expf(-acc[0]));
            }
        }
        if (cw == 3) {
            const u64 ep = (b == 0) ? start_up_master(ws, lane) : start_up(ws, b);
            if (lane == 0) {
                *(volatile lds_u32*)(lds_void_p)(smem + L_FLAG + 32) = (u32)ep;
                *(volatile lds_u32*)(lds_void_p)(smem + L_FLAG + 36) = (u32)(ep >> 32);
                asm volatile("s_waitcnt lgkmcnt(0)" ::: "memory");
                lds_add(smem, L_FLAG + 28);
            }
            STAMPR(19);
            if (b == 0) start_up_echo(ws, lane, ep);
            STAMPR(20);
            lds_wait_ge(smem, L_FLAG + 16, 9u);
            __builtin_amdgcn_s_sleep(POLL_DELAY);
            STAMPR(16);
            poll_flags<4>(ws, F_KV + (b & 7) * 256 + lane * 4, ep);
            STAMPR(17);
        }
        const int d0 = 64 * hg + 16 * cw + 2 * (lane >> 3);
        u32x4 sw;
        asm volatile("global_load_dwordx4 %0, %1, off" : "=&v"(sw) : "v"(shift_k + d0 * 4) : "memory");
        STAMP(9);
        BAR();
        {
            const int t = tid - 512;
            if (t >= 152) {
                const int sl = (t - 152) & 31;
                const u32 v = (sl == 0 || sl == 4) ? 0x3f803f80u : 0u;
                *(u32x4*)(smem + L_ONES + (t - 152) * 16) = u32x4{v, v, v, v};
            }
        }
        STAMP(2);
        asm volatile("s_waitcnt vmcnt(0)" : "+v"(sw)::"memory");
        if (lane == 0) lds_add(smem, L_FLAG + 24);
        lds_wait_ge(smem, L_FLAG + 24, 4u);
        STAMP(3);
        {
            const int tb = lane & 7, dp = lane >> 3;
            const int t0 = 64 * sb + 8 * tb;
            const int j = 2 * cw + (dp >> 2);
            u32 kr[11], vr[8];
#pragma unroll
            for (int i = 0; i < 11; ++i) {
                const int r = 8 * tb + i;
                kr[i] = *(const u32*)(smem + L_TS + r * 128 + 16 * (j ^ ((r >> 3) & 7)) + 4 * (dp & 3));
                if (i < 3 && t0 - 3 + i < 0) kr[i] = 0u;
            }
#pragma unroll
            for (int i = 0; i < 8; ++i) vr[i] = *(const u32*)(smem + L_TS + 9216 + (8 * tb + i) * 128 + 16 * (j ^ tb) + 4 * (dp & 3));
            const float w00 = bf_lo(sw[0]), w01 = bf_hi(sw[0]), w02 = bf_lo(sw[1]), w03 = bf_hi(sw[1]);
            const float w10 = bf_lo(sw[2]), w11 = bf_hi(sw[2]), w12 = bf_lo(sw[3]), w13 = bf_hi(sw[3]);
            float u0[8], u1[8];
#pragma unroll
            for (int i = 0; i < 8; ++i) {
                const float a = bf_lo(kr[i + 3]) * w00 + bf_lo(kr[i + 2]) * w01 + bf_lo(kr[i + 1]) * w02 + bf_lo(kr[i]) * w03;
                const float c = bf_hi(kr[i + 3]) * w10 + bf_hi(kr[i + 2]) * w11 + bf_hi(kr[i + 1]) * w12 + bf_hi(kr[i]) * w13;
                u0[i] = a * bf_lo(vr[i]);
                u1[i] = c * bf_hi(vr[i]);
            }
            const int i0 = 2040 - t0;
            u32x4 o0 = {pk_bf16(u0[7], u0[6]), pk_bf16(u0[5], u0[4]), pk_bf16(u0[3], u0[2]), pk_bf16(u0[1], u0[0])};
            u32x4 o1 = {pk_bf16(u1[7], u1[6]), pk_bf16(u1[5], u1[4]), pk_bf16(u1[3], u1[2]), pk_bf16(u1[1], u1[0])};
            const __amdgpu_buffer_rsrc_t ur = __builtin_amdgcn_make_buffer_rsrc(ws + WS_UR, 0, 2097152, 0x00020000);
            __builtin_amdgcn_raw_buffer_store_b128(o0, ur, (u32)((d0 * SEQ + i0) * 2), 0, 16);
            __builtin_amdgcn_raw_buffer_store_b128(o1, ur, (u32)(((d0 + 1) * SEQ + i0) * 2), 0, 16);
        }
        const u16* hrow = ssm_k + (size_t)chg * SEQ;
        u32x4 hn[3];
        u32 hn4[3];
        u32 dv16;
        {
            const u32x4 srd = {(u32)(size_t)hrow, (u32)((size_t)hrow >> 32) & 0xffffu, 4096u, 0x00020000u};
#pragma unroll
            for (int p = 0; p < 3; ++p) {
                const int hi = 130 * (cw >> 1) + 64 * p + lane;
                const u32 off = (u32)(16 * (hi - 4));
                asm volatile("buffer_load_dwordx4 %0, %2, %3, 0 offen\n\tbuffer_load_dword %1, %2, %3, 0 offen offset:16"
                             : "=&v"(hn[p]), "=&v"(hn4[p]) : "v"(off), "s"(srd) : "memory");
            }
            asm volatile("global_load_ushort %0, %1, off" : "=&v"(dv16) : "v"(ssm_D + chg) : "memory");
        }
        BAR();
        STAMP(4);
        {
            const char* kg_ = ws + WS_KF + lane * 16;
            const char* vg_ = ws + WS_VF + lane * 16;
#pragma unroll
            for (int i = 0; i < 13; ++i) {
                const int T = 12 + i * 4 + cw;
                dma16_dev(kg_ + T * 1024, smem + L_K + T * 1024);
                dma16_dev(vg_ + T * 1024, smem + L_V + T * 1024);
            }
        }
        asm volatile("s_waitcnt vmcnt(26)"
                     : "+v"(hn[0]), "+v"(hn[1]), "+v"(hn[2]), "+v"(hn4[0]), "+v"(hn4[1]), "+v"(hn4[2]), "+v"(dv16)::"memory");
        if (tid - 512 < 160) *(u32x4*)(smem + L_U + 64 * 80 + (tid - 512) * 16) = u32x4{0, 0, 0, 0};
#pragma unroll
        for (int p = 0; p < 3; ++p) {
            const int hi = 130 * (cw >> 1) + 64 * p + lane;
            if (p < 2 || lane < 2) {
                u32* cE = (u32*)(smem + L_H + ch * 8320) + 4 * hi;
                u32* cO = cE + 1040;
                u32x4 e = {hn[p][1], hn[p][2], hn[p][3], hn4[p]};
                u32x4 o = {__builtin_amdgcn_alignbit(hn[p][1], hn[p][0], 16), __builtin_amdgcn_alignbit(hn[p][2], hn[p][1], 16),
                           __builtin_amdgcn_alignbit(hn[p][3], hn[p][2], 16), __builtin_amdgcn_alignbit(hn4[p], hn[p][3], 16)};
                *(u32x4*)cE = e;
                *(u32x4*)cO = o;
            }
        }
        DRAIN();
        if (lane == 0) lds_add(smem, L_FLAG);
        if (cw == 3) {
            lds_wait_ge(smem, L_FLAG, 4u);
            if (lane < 4) flag_set(ws, F_T + (hg * 4 + lane) * 32 + sb, lds_epoch(smem));
            poll_flags<1>(ws, F_T + ((b >> 5) * 4 + (b & 3)) * 32 + (lane & 31), lds_epoch(smem));
            if (lane == 0) lds_add(smem, L_FLAG + 8);
        }
        lds_wait_ge(smem, L_FLAG + 8, 1u);
        {
            const u16* ug = (const u16*)(ws + WS_UR) + (size_t)chg * SEQ;
#pragma unroll
            for (int i = 0; i < 3; ++i) {
                const int pc = (cw >> 1) ? (i == 2 ? 4 : 3 + i) : i;
                const int z = 64 * pc + lane;
                const int P = z / 5;
                int sg = z - 5 * P;
                if (sg == 4) sg = 0;
                const int beta = ch == 0 ? 63 - P : P;
                dma16_dev(ug + 2016 - 32 * beta + 8 * sg, smem + L_U + (ch == 0 ? 0 : 96 * 80) + pc * 1024);
            }
        }
        asm volatile("s_waitcnt vmcnt(0) lgkmcnt(0)" ::: "memory");
        if (lane == 0) lds_add(smem, L_FLAG + 12);
        lds_wait_ge(smem, L_FLAG + 12, 4u);
        STAMP(5);
        {
            const float Dv = bf_lo(dv16);
            if (ch) conv_wave<1>(smem, cw >> 1, lane, chg, Dv, ws);
            else conv_wave<0>(smem, cw >> 1, lane, chg, Dv, ws);
        }
        DRAIN();
        if (lane == 0) lds_add(smem, L_FLAG + 4);
        STAMP(6);
        __builtin_amdgcn_s_setprio(3);
        if (cw == 3) {
            lds_wait_ge(smem, L_FLAG + 4, 4u);
            if (lane < 4) flag_set(ws, F_Y + ((b >> 5) * 4 + lane) * 32 + (b & 31), lds_epoch(smem));
            poll_flags<1>(ws, F_Y + (hg * 4 + (sb & 3)) * 32 + (lane & 31), lds_epoch(smem));
        }
        STAMPR(13);
        BAR();
        STAMPR(14);
        if (cw == 3) poll_flags<1>(ws, F_O + sb * 64 + lane, lds_epoch(smem));
        STAMPR(15);
        STAMP(7);
        BAR();
        BAR();
        STAMP(8); STAMPR(23); STAMP_FLUSH();
        return;
    }

    const int st = w & 1;
    const int r31 = lane & 31;
    const int g4 = lane >> 4, c16 = lane & 15, q4 = c16 >> 2, p4 = lane & 3;
    {
        const int kg = hg >> 1;
        __builtin_amdgcn_s_sleep(HEAD_START);
        const u16* xg = x + (size_t)(64 * sb) * DM + lane * 8;
#pragma unroll
        for (int i = 0; i < 6; ++i) {
            int row = i * 8 + w + 6 * hg;
            if (row >= 48) row -= 48;
            if (row >= 16 * kg) row += 16;
            dma16(xg + row * DM, smem + L_X + row * XP);
        }
        const u16* wg = wq + hg * 64;
#pragma unroll
        for (int i = 0; i < 8; ++i) {
            const int k0 = ((i * 8 + w + 16 * (b >> 6)) & 63) * 8;
            const int k = k0 + (lane >> 3);
            const int cp = lane & 7;
            dma16(wg + k * DM + 8 * (cp ^ (4 * ((k >> 1) & 1)) ^ (2 * ((k >> 3) & 1))), smem + L_W + k0 * 128);
        }
    }
    STAMP(1);
    DRAIN();
    if (lane == 0) lds_add(smem, L_FLAG + 16);
    STAMP(2);
    lds_wait_ge(smem, L_FLAG + 16, 9u);
    STAMP(3);
    u32x4 qf;
    {
        const int hl = w >> 1;
        const u32 aaddr = lds0 + L_W + (8 * g4 + q4) * 128 + 16 * ((4 * (hl >> 1) + 2 * (hl & 1) + (p4 >> 1)) ^ (4 * ((q4 >> 1) & 1)) ^ (2 * (g4 & 1))) + 8 * (p4 & 1);
        const u32 baddr = lds0 + L_X + (32 * st + c16) * XP + 16 * g4;
        f32x4 c0, c1;
        gemm_proj(aaddr, baddr, c0, c1);
        const float sc = 0.25f * 1.4426950408889634f;
        c0 *= sc;
        c1 *= sc;
        u32 a0 = pk_bf16(c0[0], c0[1]), a1 = pk_bf16(c0[2], c0[3]), b0 = pk_bf16(c1[0], c1[1]), b1 = pk_bf16(c1[2], c1[3]);
        auto s0 = __builtin_amdgcn_permlane16_swap(a0, b0, false, false);
        auto s1 = __builtin_amdgcn_permlane16_swap(a1, b1, false, false);
        auto t0 = __builtin_amdgcn_permlane32_swap((u32)s0[0], (u32)s0[1], false, false);
        auto t1 = __builtin_amdgcn_permlane32_swap((u32)s1[0], (u32)s1[1], false, false);
        qf = u32x4{(u32)t0[0], (u32)t1[0], (u32)t0[1], (u32)t1[1]};
    }
    STAMP(4);
    float q2 = 0.f;
#pragma unroll
    for (int i = 0; i < 4; ++i) q2 += bf_lo(qf[i]) * bf_lo(qf[i]) + bf_hi(qf[i]) * bf_hi(qf[i]);
    q2 = add_halves(q2);
    STAMP(5);
    BAR();
    STAMP(6);
    {
        const char* kg_ = ws + WS_KF + lane * 16;
        const char* vg_ = ws + WS_VF + lane * 16;
#pragma unroll
        for (int i = 0; i < 3; ++i) {
            const int c = w * 3 + i;
            const int T = c >> 1;
            if (c & 1) dma16_dev(vg_ + T * 1024, smem + L_V + T * 1024);
            else dma16_dev(kg_ + T * 1024, smem + L_K + T * 1024);
        }
    }
    const u64 k2 = __hip_atomic_load((gu64*)(ws + WS_KM) + lane, RLX_AGENT);
    const float km = wave_max(fmaxf(__builtin_bit_cast(float, (u32)k2), __builtin_bit_cast(float, (u32)(k2 >> 32))));
    float mrow = sqrtf(q2 * km) * 1.002f + 1e-6f;
    DRAIN();
    BAR();
    STAMP(7);
    int wait_iter = 2;
    if (!__all(mrow <= 48.f)) {
        lds_wait_ge(smem, L_FLAG, 4u);
        wait_iter = -1;
        float mx = -3.0e38f;
        for (int T = 0; T < 64; ++T) {
            const bf16x8 kf = as_bf8(lds_rd128(smem + L_K + T * 1024 + lane * 16));
            f32x16 s = {0};
            s = __builtin_amdgcn_mfma_f32_32x32x16_bf16(kf, as_bf8(qf), s, 0, 0, 0);
#pragma unroll
            for (int i = 0; i < 16; ++i) mx = fmaxf(mx, s[i]);
        }
        mrow = max_halves(mx);
    }
    wait_iter = __builtin_amdgcn_readfirstlane(wait_iter);
    f32x16 O;
    {
        const bool cst = r31 >= 16;
        const int cslot = r31 == 16 ? 0 : (r31 == 20 ? 4 : ((r31 > 20 && r31 < 28) ? 5 : 1));
        const u32 kaddr = lds0 + L_K + lane * 16;
        const u32 va0 = cst ? (lds0 + L_ONES + cslot * 16) : (lds0 + L_V + hh * 256 + r31 * 16);
        const u32 vinc = cst ? 0u : 4096u;
        const u32 va1 = cst ? va0 : va0 + 2048;
        O = attn_loop(qf, -mrow, kaddr, va0, va1, vinc, lds0 + L_FLAG, wait_iter, (w >> 2) ? PRIO_IT : -1);
    }
    STAMP(8); STAMPR(13);
    {
        const float inv = 1.f / O[8];
        u32x4 o = {pk_bf16(O[0] * inv, O[1] * inv), pk_bf16(O[2] * inv, O[3] * inv), pk_bf16(O[4] * inv, O[5] * inv),
                   pk_bf16(O[6] * inv, O[7] * inv)};
        const int head = 4 * hg + (w >> 1);
        const __amdgpu_buffer_rsrc_t orr = __builtin_amdgcn_make_buffer_rsrc(ws + WS_OF, 0, 2097152, 0x00020000);
        __builtin_amdgcn_raw_buffer_store_b128(o, orr, (u32)(((head * 64 + 2 * sb + st) * 64 + lane) * 16), 0, 16);
    }
    DRAIN();
    if (lane == 0) flag_set(ws, F_O + sb * 64 + hg * 8 + w, lds_epoch(smem));
    BAR();
    STAMP(9);
    {
        const u16* wg = wo + hg * 64;
#pragma unroll
        for (int i = 0; i < 8; ++i) {
            const int k0_ = (w * 8 + i) * 8;
            const int k = k0_ + (lane >> 3);
            const int cp = lane & 7;
            dma16(wg + k * DM + 8 * (cp ^ (4 * ((k >> 1) & 1)) ^ (2 * ((k >> 2) & 1))), smem + L_WO + k0_ * 128);
        }
    }
    const int nq = w >> 1;
    const int s0 = 64 * sb + 32 * st + c16;
    const int n0 = 64 * hg + 16 * nq + 4 * g4;
    u32x2 xa, xb2;
    u32 yv[8];
    {
        const u16* xp = x + (size_t)s0 * DM + n0;
        asm volatile("global_load_dwordx2 %0, %2, off\n\tglobal_load_dwordx2 %1, %3, off" : "=&v"(xa), "=&v"(xb2) : "v"(xp), "v"(xp + 16 * DM) : "memory");
        const u16* yp = (const u16*)(ws + WS_YT) + (size_t)n0 * SEQ + s0;
#pragma unroll
        for (int i = 0; i < 8; ++i)
            asm volatile("global_load_ushort %0, %1, off sc1" : "=&v"(yv[i]) : "v"(yp + (i & 3) * SEQ + 16 * (i >> 2)) : "memory");
    }
    BAR();
    STAMP(10);
    {
#pragma unroll
        for (int i = 0; i < 8; ++i) {
            const int c = w * 8 + i;
            dma16_dev(ws + WS_OF + ((size_t)((c >> 1) * 64 + 2 * sb + (c & 1)) * 64 + lane) * 16, smem + L_O + c * 1024);
        }
    }
    const float ga = ((const float*)(smem + L_G))[32 * st + c16], gb = ((const float*)(smem + L_G))[32 * st + 16 + c16];
    asm volatile("s_waitcnt vmcnt(0)"
                 : "+v"(xa), "+v"(xb2), "+v"(yv[0]), "+v"(yv[1]), "+v"(yv[2]), "+v"(yv[3]), "+v"(yv[4]), "+v"(yv[5]), "+v"(yv[6]), "+v"(yv[7])
                 :: "memory");
    BAR();
    STAMP(11);
    {
        const int kr = 16 * (g4 >> 1) + 4 * (g4 & 1) + q4;
        const u32 aaddr = lds0 + L_WO + kr * 128 + 16 * ((2 * nq + (p4 >> 1)) ^ (4 * ((kr >> 1) & 1)) ^ (2 * ((kr >> 2) & 1))) + 8 * (p4 & 1);
        const u32 baddr = lds0 + L_O + (2 * (g4 >> 1) + st) * 1024 + (32 * (g4 & 1) + c16) * 16;
        f32x4 c0, c1;
        gemm_out(aaddr, baddr, c0, c1);
        const float ga1 = 1.f - ga, gb1 = 1.f - gb;
        const u32 a0 = pk_bf16(ga * bf_lo(xa[0]) * bf_lo(yv[0]) + ga1 * c0[0], ga * bf_hi(xa[0]) * bf_lo(yv[1]) + ga1 * c0[1]);
        const u32 a1 = pk_bf16(ga * bf_lo(xa[1]) * bf_lo(yv[2]) + ga1 * c0[2], ga * bf_hi(xa[1]) * bf_lo(yv[3]) + ga1 * c0[3]);
        const u32 b0 = pk_bf16(gb * bf_lo(xb2[0]) * bf_lo(yv[4]) + gb1 * c1[0], gb * bf_hi(xb2[0]) * bf_lo(yv[5]) + gb1 * c1[1]);
        const u32 b1 = pk_bf16(gb * bf_lo(xb2[1]) * bf_lo(yv[6]) + gb1 * c1[2], gb * bf_hi(xb2[1]) * bf_lo(yv[7]) + gb1 * c1[3]);
        auto r0 = __builtin_amdgcn_permlane16_swap(a0, b0, false, false);
        auto r1 = __builtin_amdgcn_permlane16_swap(a1, b1, false, false);
        u32x4 ov = {(u32)r0[0], (u32)r1[0], (u32)r0[1], (u32)r1[1]};
        *(u32x4*)(out + (size_t)(s0 + 16 * (g4 & 1)) * DM + 64 * hg + 16 * nq + 8 * (g4 >> 1)) = ov;
    }
    STAMP(12); STAMPR(23); STAMP_FLUSH();
}

extern "C" void kernel_launch(void* const* d_in, const int* in_sizes, int n_in, void* d_out, int out_size, void* d_ws,
                              size_t ws_size, hipStream_t stream) {
    const u16* A_q = (const u16*)d_in[0];
    const u16* A_k = (const u16*)d_in[1];
    const u16* A_v = (const u16*)d_in[2];
    const u16* shift_k = (const u16*)d_in[3];
    const u16* ssm_k = (const u16*)d_in[4];
    const u16* ssm_D = (const u16*)d_in[5];
    const u16* wq = (const u16*)d_in[7];
    const u16* wk = (const u16*)d_in[8];
    const u16* wv = (const u16*)d_in[9];
    const u16* wo = (const u16*)d_in[10];
    const u16* gate_w = (const u16*)d_in[11];
    char* ws = (char*)d_ws;
    (void)hipFuncSetAttribute((const void*)k_mega, hipFuncAttributeMaxDynamicSharedMemorySize, LDS_BYTES);
    k_mega<<<256, 768, LDS_BYTES, stream>>>(A_q, A_k, A_v, shift_k, ssm_k, ssm_D, wq, wk, wv, wo, gate_w, ws, (u16*)d_out);
}
```
